```python
import torch

def _softmax(x, axis=-1):
    return torch.softmax(x, dim=axis)
def _gelu(x):
    return torch.nn.functional.gelu(x, approximate="tanh")
def _silu(x):
    return torch.nn.functional.silu(x)
def _sigmoid(x):
    return torch.sigmoid(x)
def _layernorm(x, axis=-1, eps=1e-5):
    m = x.mean(dim=axis, keepdim=True)
    v = x.var(dim=axis, keepdim=True, unbiased=False)
    return (x - m) / torch.sqrt(v + eps)
def _scatter_add(tgt, idx, src, axis=0):
    return tgt.clone().index_add_(axis, idx, src)
def _segment_sum(src, seg, n):
    out = torch.zeros((n,) + src.shape[1:], dtype=src.dtype)
    return out.index_add_(0, seg, src)
def _pad(x, pw):
    flat = tuple(v for pair in reversed(pw) for v in pair)
    return torch.nn.functional.pad(x, flat)

kernel_name = 'recurrent_scan__softmax_full_mha__mixer_channel_mlp__prompt'

def setup_inputs():

    _w = (lambda a: (lambda t: t.to(torch.bfloat16) if t.is_floating_point() else t)(torch.as_tensor(a)))
    def _A_setup():
        B, S, D = 1, 2048, 640
        return {
            "x": _w((lambda *s: torch.randn(*s, dtype=torch.bfloat16) * 0.05)(B, S, D)),
            "wqkv": _w((lambda *s: torch.randn(*s, dtype=torch.bfloat16) * 0.05)(D, 3 * D)),
            "wo": _w((lambda *s: torch.randn(*s, dtype=torch.bfloat16) * 0.05)(D, D)),
        }
    _A_in = _A_setup()

    def _B_setup():
        B, S, D, F = 1, 2048, 640, 1728
        return {
            "x": _w((lambda *s: torch.randn(*s, dtype=torch.bfloat16) * 0.05)(B, S, D)),
            "w1": _w((lambda *s: torch.randn(*s, dtype=torch.bfloat16) * 3.2)(D, F) * (D ** -0.5)),
            "b1": _w((lambda *s: torch.randn(*s, dtype=torch.bfloat16) * 1.6)(F)),
            "w2": _w((lambda *s: torch.randn(*s, dtype=torch.bfloat16) * 0.05)(F, D) * (F ** -0.5)),
            "b2": _w((lambda *s: torch.randn(*s, dtype=torch.bfloat16) * 0.05)(D)),
        }
    _B_in = _B_setup()

    return {
        "A_x": _A_in["x"],
        "A_wqkv": _A_in["wqkv"],
        "A_wo": _A_in["wo"],
        "B_x": _B_in["x"],
        "B_w1": _B_in["w1"],
        "B_b1": _B_in["b1"],
        "B_w2": _B_in["w2"],
        "B_b2": _B_in["b2"],
    }

def reference(A_x, A_wqkv, A_wo, B_x, B_w1, B_b1, B_w2, B_b2):
    def _A_ref(x, wqkv, wo):
        B, S, D = x.shape
        H = 32; Dh = D // H
        qkv = (lambda x, *d: x.permute(*d))((x @ wqkv).reshape(B, S, 3, H, Dh), 2, 0, 3, 1, 4)
        q, k, v = qkv[0], qkv[1], qkv[2]
        scale = Dh ** -0.5
        scores = torch.einsum("bhqd,bhkd->bhqk", q, k) * scale
        p = _softmax(scores, axis=-1)
        o = torch.einsum("bhqk,bhkd->bhqd", p, v)
        return (lambda x, *d: x.permute(*d))(o, 0, 2, 1, 3).reshape(B, S, D) @ wo

    def _B_ref(x, w1, b1, w2, b2):
        y = _layernorm(x, axis=-1)
        h = _gelu(torch.einsum("bsd,df->bsf", y, w1) + b1)
        return torch.einsum("bsf,fd->bsd", h, w2) + b2

    x = A_x
    xn = _layernorm(x, axis=-1)
    s = _A_ref(xn, A_wqkv, A_wo)
    g = _silu(_B_ref(xn, B_w1, B_b1, B_w2, B_b2))
    return x + s * g
```

```cpp
#include <hip/hip_runtime.h>
#include <hip/hip_bf16.h>
#include <stdint.h>

typedef short bf16x8 __attribute__((ext_vector_type(8)));
typedef short bf16x4 __attribute__((ext_vector_type(4)));
typedef float f32x16 __attribute__((ext_vector_type(16)));
typedef float f32x4 __attribute__((ext_vector_type(4)));
typedef float f32x2 __attribute__((ext_vector_type(2)));
typedef unsigned u32x4 __attribute__((ext_vector_type(4)));
typedef unsigned u32x2 __attribute__((ext_vector_type(2)));
typedef __bf16 bf16x2_t __attribute__((ext_vector_type(2)));
typedef short v4i16_t __attribute__((ext_vector_type(4)));
#define LAS __attribute__((address_space(3)))
typedef LAS char* lds_ptr;

constexpr int S_ = 2048, D_ = 640, F_ = 1728, NH = 32;
constexpr float LN_EPS = 1e-5f;
constexpr float LOG2E = 1.4426950408889634f;
constexpr float QSCALE = 0.22360679774997896f * LOG2E;

constexpr size_t QT_B = 1280, KVT_B = 3072;
constexpr size_t WS_Q = 0;
constexpr size_t WS_KV = WS_Q + (size_t)NH * 64 * QT_B;
constexpr size_t WS_H = WS_KV + (size_t)NH * 64 * KVT_B;
constexpr size_t WS_O = WS_H + (size_t)64 * 108 * 1024;
constexpr size_t WS_G = WS_O + (size_t)64 * 40 * 1024;
constexpr size_t WS_W2 = WS_G + (size_t)S_ * D_ * 2;
constexpr size_t WS_WO = WS_W2 + (size_t)20 * 108 * 1024;
constexpr size_t WS_END = WS_WO + (size_t)20 * 40 * 1024;

#define MFMA16(a, b, c) __builtin_amdgcn_mfma_f32_32x32x16_bf16(a, b, c, 0, 0, 0)
#define MFMA8(a, b, c) __builtin_amdgcn_mfma_f32_32x32x8bf16_1k(a, b, c, 0, 0, 0)
#define SBAR() __builtin_amdgcn_sched_barrier(0)
#define PIN(x) asm volatile("" : "+v"(x))
#define RFL(x) __builtin_amdgcn_readfirstlane(x)

__device__ __forceinline__ unsigned pk2(float a, float b) { f32x2 v = {a, b}; return __builtin_bit_cast(unsigned, __builtin_convertvector(v, bf16x2_t)); }
__device__ __forceinline__ float bf_lo(unsigned u) { return __uint_as_float(u << 16); }
__device__ __forceinline__ float bf_hi(unsigned u) { return __uint_as_float(u & 0xffff0000u); }
__device__ __forceinline__ float xhalf_sum(float v) {
    auto r = __builtin_amdgcn_permlane32_swap(__float_as_uint(v), __float_as_uint(v), false, false);
    return __uint_as_float(r[0]) + __uint_as_float(r[1]); }
__device__ __forceinline__ float xhalf_max(float v) {
    auto r = __builtin_amdgcn_permlane32_swap(__float_as_uint(v), __float_as_uint(v), false, false);
    return __builtin_fmaxf(__uint_as_float(r[0]), __uint_as_float(r[1])); }
__device__ __forceinline__ void glds16(unsigned voff, const void* sbase, unsigned lds_base) {
    unsigned sv; asm volatile("s_mov_b32 %0, m0\n\ts_mov_b32 m0, %3\n\ts_nop 0\n\tglobal_load_lds_dwordx4 %1, %2\n\ts_mov_b32 m0, %0"
                              : "=&s"(sv) : "v"(voff), "s"(sbase), "s"(lds_base) : "memory"); }
__device__ __forceinline__ bf16x8 lds_r128(lds_ptr p) { return *(const LAS bf16x8*)p; }
__device__ __forceinline__ bf16x4 lds_r64(lds_ptr p) { return *(const LAS bf16x4*)p; }
__device__ __forceinline__ bf16x4 lds_tr(lds_ptr p) { return __builtin_bit_cast(bf16x4, __builtin_amdgcn_ds_read_tr16_b64_v4i16((LAS v4i16_t*)p)); }
__device__ __forceinline__ bf16x8 cat8(bf16x4 a, bf16x4 b) { return (bf16x8){a[0], a[1], a[2], a[3], b[0], b[1], b[2], b[3]}; }
#define WAITV_BAR(N) asm volatile("s_waitcnt vmcnt(" #N ") lgkmcnt(0)\n\ts_barrier" ::: "memory")
#define BAR_LGKM() asm volatile("s_waitcnt lgkmcnt(0)\n\ts_barrier" ::: "memory")
#define EX2(v) __builtin_amdgcn_exp2f(v)
#define RCP(v) __builtin_amdgcn_rcpf(v)

constexpr int K1_STAGE = 49152, K1_NST = 3, K1_STATS = K1_STAGE * K1_NST, K1_LDS = K1_STATS + 4 * 1024 + 2 * 512 + 256;
constexpr int K1_GEMM_BLOCKS = 232;

template <bool VORI>
__device__ __forceinline__ void k1_main(lds_ptr lds, int wf, int wt, int lane, f32x16 (&acc)[2][2], float& sx, float& sxx, float& cs) {
    const int r32 = lane & 31, h = lane >> 5, kb = wt >> 1;
    unsigned xo[4][2], wa[2], wb[2];
#pragma unroll
    for (int jp = 0; jp < 2; ++jp) { const int row = 64 * wt + 32 * (jp ^ wf) + r32;
#pragma unroll
        for (int ks = 0; ks < 4; ++ks) xo[ks][jp] = row * 128 + (((2 * (ks ^ (2 * kb)) + h) ^ ((row >> 1) & 7)) << 4); }
    { const int blk = (lane >> 4) & 1, q = (lane & 15) >> 2, p = lane & 3;
#pragma unroll
      for (int ip = 0; ip < 2; ++ip) { const int ch = 4 * (ip ^ (wt & 1)) + 2 * blk + (p >> 1);
          const unsigned a = 32768 + wf * 8192 + (8 * h + q) * 128 + 16 * (ch ^ ((q >> 1) << 2)) + 8 * (p & 1);
          wa[ip] = a + kb * 4096; wb[ip] = a + (kb ^ 1) * 4096; } }
#pragma unroll
    for (int a = 0; a < 2; ++a)
#pragma unroll
        for (int b = 0; b < 2; ++b) acc[a][b] = f32x16{};
    f32x2 sx2 = {0.f, 0.f}, sq2 = {0.f, 0.f}, cs2 = {0.f, 0.f};
    int slot = 0;
#pragma unroll 1
    for (int s = 0; s < 10; ++s) {
        BAR_LGKM();
        const lds_ptr st = lds + slot * K1_STAGE; slot = slot == K1_NST - 1 ? 0 : slot + 1;
#pragma unroll
        for (int ks = 0; ks < 4; ++ks) {
            const bf16x8 x0 = lds_r128(st + xo[ks][0]), x1 = lds_r128(st + xo[ks][1]);
            const lds_ptr p0 = st + (ks < 2 ? wa[0] : wb[0]) + (ks & 1) * 2048, p1 = st + (ks < 2 ? wa[1] : wb[1]) + (ks & 1) * 2048;
            const bf16x8 w0 = cat8(lds_tr(p0), lds_tr(p0 + 512)), w1 = cat8(lds_tr(p1), lds_tr(p1 + 512));
            if (VORI) { acc[0][0] = MFMA16(x0, w0, acc[0][0]); acc[0][1] = MFMA16(x1, w0, acc[0][1]); acc[1][0] = MFMA16(x0, w1, acc[1][0]); acc[1][1] = MFMA16(x1, w1, acc[1][1]); }
            else      { acc[0][0] = MFMA16(w0, x0, acc[0][0]); acc[0][1] = MFMA16(w0, x1, acc[0][1]); acc[1][0] = MFMA16(w1, x0, acc[1][0]); acc[1][1] = MFMA16(w1, x1, acc[1][1]); }
            const u32x4 xu = __builtin_bit_cast(u32x4, x0), wu = __builtin_bit_cast(u32x4, w0);
#pragma unroll
            for (int j = 0; j < 4; ++j) { const f32x2 v = {bf_lo(xu[j]), bf_hi(xu[j])}; sx2 += v; sq2 = __builtin_elementwise_fma(v, v, sq2); }
            if (ks < 2) {
#pragma unroll
                for (int j = 0; j < 4; ++j) { const f32x2 v = {bf_lo(wu[j]), bf_hi(wu[j])}; cs2 += v; } }
        }
    }
    sx = xhalf_sum(sx2[0] + sx2[1]); sxx = xhalf_sum(sq2[0] + sq2[1]); cs = xhalf_sum(cs2[0] + cs2[1]);
}

__device__ __forceinline__ void k1_compute(lds_ptr lds, int wid, int lane, int row0, int type, int grp, const unsigned short* x, char* ws) {
    const int wf = wid >> 2, wt = wid & 3, r32 = lane & 31, h = lane >> 5;
    f32x16 acc[2][2]; float sx, sxx, cs;
    if (type == 1) k1_main<true>(lds, wf, wt, lane, acc, sx, sxx, cs); else k1_main<false>(lds, wf, wt, lane, acc, sx, sxx, cs);
    LAS float* s_r = (LAS float*)(lds + K1_STATS); LAS float* s_rm = s_r + 256; LAS float* s_a2 = s_r + 512; LAS float* s_m2 = s_r + 768;
    LAS float* s_cs = s_r + 1024; LAS float* s_b1 = s_r + 1280;
    { const int tok = 64 * wt + 32 * wf + r32;
      const float m = sx * (1.0f / D_);
      float var = __builtin_fmaf(-m, m, sxx * (1.0f / D_));
      if (__builtin_expect(__any(!(m * m <= 64.0f * var)), 0)) {
          const unsigned short* row = x + (size_t)(row0 + tok) * D_; float a = 0.f;
#pragma unroll 1
          for (int c = 0; c < D_ / 8; ++c) { const u32x4 v = *(const u32x4*)(row + c * 8);
#pragma unroll
              for (int j = 0; j < 4; ++j) { const float lo = bf_lo(v[j]) - m, hi = bf_hi(v[j]) - m; a = __builtin_fmaf(lo, lo, a); a = __builtin_fmaf(hi, hi, a); } }
          var = a * (1.0f / D_);
      }
      const float r = __builtin_amdgcn_rsqf(var + LN_EPS);
      const float a2 = r * __builtin_amdgcn_rsqf(__builtin_fmaf(var * r, r, LN_EPS));
      if (h == 0) { s_r[tok] = r; s_rm[tok] = r * m; s_a2[tok] = a2; s_m2[tok] = a2 * m; s_cs[(wt >> 1) * 128 + 64 * wf + 32 * (wt & 1) + r32] = cs; } }
    BAR_LGKM();
    if (type == 0) {
        float rr[2], rm[2];
        const bool isq = grp < 10;
#pragma unroll
        for (int jp = 0; jp < 2; ++jp) { const int t = 64 * wt + 32 * (jp ^ wf) + r32; const float sc = isq ? QSCALE : 1.0f; rr[jp] = s_r[t] * sc; rm[jp] = -s_rm[t] * sc; }
        char* base = isq ? ws + WS_Q : ws + WS_KV; const int tb = isq ? (int)QT_B : (int)KVT_B;
#pragma unroll
        for (int ip = 0; ip < 2; ++ip)
#pragma unroll
            for (int g = 0; g < 4; ++g) {
                const int fl = 32 * (ip ^ (wt & 1)) + 8 * g + 4 * h, f0 = 64 * (isq ? grp : grp - 10) + fl;
                const f32x4 c4 = *(const LAS f32x4*)(s_cs + 64 * wf + fl) + *(const LAS f32x4*)(s_cs + 128 + 64 * wf + fl);
                const int hd = (f0 * 3277) >> 16, d0 = f0 - hd * 20;
                const int off = hd * 64 * tb + (d0 < 16 ? (d0 >> 3) * 512 + r32 * 16 + (d0 & 4) * 2 : 1024 + r32 * 8);
#pragma unroll
                for (int jp = 0; jp < 2; ++jp) {
                    const int tile = (row0 >> 5) + 2 * wt + (jp ^ wf);
                    float v[4];
#pragma unroll
                    for (int e = 0; e < 4; ++e) v[e] = __builtin_fmaf(acc[ip][jp][4 * g + e], rr[jp], rm[jp] * c4[e]);
                    char* p = base + off + tile * tb;
                    *(u32x2*)p = (u32x2){pk2(v[0], v[1]), pk2(v[2], v[3])};
                    if (!isq && d0 == 16) *(u32x2*)(p + 256) = (u32x2){0x00003F80u, 0u};
                }
            }
    } else if (type == 1) {
#pragma unroll
        for (int ip = 0; ip < 2; ++ip) {
            const int fl = 32 * (ip ^ (wt & 1)) + r32, f = 64 * (grp - 20) + fl;
            const int hd = (f * 3277) >> 16, d = f - hd * 20;
            const float c = -(s_cs[64 * wf + fl] + s_cs[128 + 64 * wf + fl]);
#pragma unroll
            for (int jp = 0; jp < 2; ++jp) {
                const int tl = 64 * wt + 32 * (jp ^ wf);
                char* base = ws + WS_KV + (size_t)(hd * 64 + ((row0 + tl) >> 5)) * KVT_B + 1536 + (h * 24 + d) * 16;
#pragma unroll
                for (int s = 0; s < 2; ++s) {
                    u32x4 o;
#pragma unroll
                    for (int gg = 0; gg < 2; ++gg) { const int g = 2 * s + gg;
                        const f32x4 r4 = *(const LAS f32x4*)(s_r + tl + 8 * g + 4 * h), m4 = *(const LAS f32x4*)(s_rm + tl + 8 * g + 4 * h);
                        float v[4];
#pragma unroll
                        for (int e = 0; e < 4; ++e) v[e] = __builtin_fmaf(acc[ip][jp][4 * g + e], r4[e], m4[e] * c);
                        o[2 * gg] = pk2(v[0], v[1]); o[2 * gg + 1] = pk2(v[2], v[3]); }
                    *(u32x4*)(base + s * 768) = o;
                    if (d == 19) *(u32x4*)(base + s * 768 + 16) = (u32x4){0x3F803F80u, 0x3F803F80u, 0x3F803F80u, 0x3F803F80u};
                }
            }
        }
    } else if (type == 2) {
        constexpr float C1 = -2.0f * LOG2E * 0.7978845608028654f, C2 = C1 * 0.044715f;
        float a2[2], m2[2];
#pragma unroll
        for (int jp = 0; jp < 2; ++jp) { const int t = 64 * wt + 32 * (jp ^ wf) + r32; a2[jp] = s_a2[t]; m2[jp] = -s_m2[t]; }
#pragma unroll
        for (int ip = 0; ip < 2; ++ip)
#pragma unroll
            for (int s = 0; s < 2; ++s) {
                u32x4 o[2];
#pragma unroll
                for (int gg = 0; gg < 2; ++gg) {
                    const int g = 2 * s + gg, fl = 32 * (ip ^ (wt & 1)) + 8 * g + 4 * h;
                    const f32x4 c4 = *(const LAS f32x4*)(s_cs + 64 + fl) + *(const LAS f32x4*)(s_cs + 128 + 64 + fl), b4 = *(const LAS f32x4*)(s_b1 + fl);
#pragma unroll
                    for (int jp = 0; jp < 2; ++jp) {
                        float v[4];
#pragma unroll
                        for (int e = 0; e < 4; ++e) {
                            const float u = __builtin_fmaf(acc[ip][jp][4 * g + e], a2[jp], __builtin_fmaf(m2[jp], c4[e], b4[e]));
                            const float t = EX2(u * __builtin_fmaf(u * u, C2, C1));
                            v[e] = u * RCP(1.0f + t);
                        }
                        o[jp][2 * gg] = pk2(v[0], v[1]); o[jp][2 * gg + 1] = pk2(v[2], v[3]);
                    }
                }
#pragma unroll
                for (int jp = 0; jp < 2; ++jp) {
                    const int tile = (row0 >> 5) + 2 * wt + (jp ^ wf);
                    *(u32x4*)(ws + WS_H + (size_t)(tile * 108 + 4 * grp + 2 * (ip ^ (wt & 1)) + s) * 1024 + lane * 16) = o[jp];
                }
            }
    }
}

__device__ __forceinline__ void k1_loader(lds_ptr lds, int lw, int lane, int row0, const unsigned short* x, const char* w0, int ld0, const char* w1, int ld1,
                                          const unsigned short* b1g) {
    const unsigned l0 = (unsigned)(uintptr_t)lds;
    unsigned xv[2];
#pragma unroll
    for (int pp = 0; pp < 2; ++pp) { const int row = 8 * pp + (lane >> 3); xv[pp] = row * (D_ * 2) + (((lane & 7) ^ ((row >> 1) & 7)) << 4); }
    const int wc = ((lane & 7) ^ (((lane >> 4) & 1) << 2)) << 4;
    const char* wsrc = lw < 2 ? w0 : w1; const int ldw = lw < 2 ? ld0 : ld1;
    const unsigned wv = (lane >> 3) * ldw + wc;
    wsrc += (size_t)(lw & 1) * 32 * ldw;
    const char* xsrc = (const char*)x + (size_t)(row0 + 64 * lw) * (D_ * 2);
    auto issue = [&](int s, int slot) {
        const unsigned base = l0 + slot * K1_STAGE;
#pragma unroll
        for (int i = 0; i < 4; ++i) {
            glds16(wv, wsrc + (size_t)(s * 64 + 8 * i) * ldw, base + 32768 + (4 * lw + i) * 1024);
            glds16(xv[0], xsrc + i * 16 * (D_ * 2) + s * 128, base + (8 * lw + 2 * i) * 1024);
            glds16(xv[1], xsrc + i * 16 * (D_ * 2) + s * 128, base + (8 * lw + 2 * i + 1) * 1024);
        }
    };
    issue(0, 0); issue(1, 1);
    if (lw == 3 && b1g) ((LAS float*)(lds + K1_STATS))[1280 + lane] = __uint_as_float((unsigned)b1g[lane] << 16);
    int slot = 2;
#pragma unroll 1
    for (int s = 0; s < 10; ++s) {
        if (s < 9) WAITV_BAR(12); else WAITV_BAR(0);
        if (s + 2 < 10) issue(s + 2, slot);
        slot = slot == K1_NST - 1 ? 0 : slot + 1;
    }
    asm volatile("s_barrier" ::: "memory");
}

__device__ __forceinline__ void k1_retile(lds_ptr lds, int gw, int wid, int lane, const unsigned short* w2, const unsigned short* wo, char* ws) {
    if (gw >= 260) return;
    const lds_ptr my = lds + wid * 12288; const unsigned l0 = (unsigned)(uintptr_t)my;
    const int h = lane >> 5, blk = (lane >> 4) & 1, q = (lane & 15) >> 2, p = lane & 3;
    const unsigned voff = (lane >> 2) * (D_ * 2) + (lane & 3) * 16;
    const unsigned ro = q * 64 + blk * 32 + p * 8;
    if (gw < 180) {
#pragma unroll
        for (int u = 0; u < 3; ++u) { const int un = 3 * gw + u, kb = un / 20, ft = un - kb * 20;
#pragma unroll
            for (int i = 0; i < 4; ++i) glds16(voff, (const char*)w2 + (size_t)(64 * kb + 16 * i) * (D_ * 2) + ft * 64, l0 + u * 4096 + i * 1024); }
        asm volatile("s_waitcnt vmcnt(0)" ::: "memory");
#pragma unroll
        for (int u = 0; u < 3; ++u) { const int un = 3 * gw + u, kb = un / 20, ft = un - kb * 20;
#pragma unroll
            for (int ks = 0; ks < 4; ++ks) { const lds_ptr a = my + u * 4096 + (16 * ks + 4 * h) * 64 + ro;
                *(bf16x8*)(ws + WS_W2 + (size_t)(ft * 108 + 4 * kb + ks) * 1024 + lane * 16) = cat8(lds_tr(a), lds_tr(a + 512)); } }
    } else {
#pragma unroll
        for (int u = 0; u < 2; ++u) { const int un = 2 * (gw - 180) + u, gp = un / 20, ft = un - gp * 20;
#pragma unroll
            for (int i = 0; i < 5; ++i) glds16(voff, (const char*)wo + (size_t)(80 * gp + 16 * i) * (D_ * 2) + ft * 64, l0 + u * 5120 + i * 1024); }
        asm volatile("s_waitcnt vmcnt(0)" ::: "memory");
#pragma unroll
        for (int u = 0; u < 2; ++u) { const int un = 2 * (gw - 180) + u, gp = un / 20, ft = un - gp * 20;
#pragma unroll
            for (int ks = 0; ks < 5; ++ks) {
                const lds_ptr a = my + u * 5120 + (ks < 4 ? 20 * ks + 4 * h : 40 * h + 16) * 64 + ro;
                *(bf16x8*)(ws + WS_WO + (size_t)(ft * 40 + 5 * gp + ks) * 1024 + lane * 16) = cat8(lds_tr(a), lds_tr(a + (ks < 4 ? 512 : 1280))); } }
    }
}

__global__ __launch_bounds__(768, 3) void k1_proj(const unsigned short* x, const unsigned short* wqkv, const unsigned short* w1, const unsigned short* b1,
                                                  const unsigned short* w2, const unsigned short* wo, char* ws) {
    extern __shared__ __attribute__((aligned(16))) char smem[];
    const lds_ptr lds = (lds_ptr)smem;
    const int lane = threadIdx.x & 63, wid = RFL(threadIdx.x >> 6);
    const int b = blockIdx.x;
    if (b >= K1_GEMM_BLOCKS) { k1_retile(lds, (b - K1_GEMM_BLOCKS) * 12 + wid, wid, lane, w2, wo, ws); return; }
    const int xcd = b & 7, t = 29 * (xcd & 3) + (b >> 3);
    const int rb = (xcd >> 2) * 4 + (t & 3), cb = t >> 2, row0 = rb * 256;
    const int g0 = cb < 28 ? cb : 29, g1 = cb < 27 ? cb : cb + 1;
    if (wid >= 8) {
        const char* wb = cb < 27 ? (const char*)w1 + g1 * 128 : (const char*)wqkv + g1 * 128;
        k1_loader(lds, wid - 8, lane, row0, x, (const char*)wqkv + g0 * 128, 3 * D_ * 2, wb, cb < 27 ? F_ * 2 : 3 * D_ * 2, cb < 27 ? b1 + g1 * 64 : nullptr);
    } else {
        const int half = wid >> 2, grp = half ? g1 : g0;
        const int type = (half && cb < 27) ? 2 : (half && cb == 28) ? 3 : grp >= 20 ? 1 : 0;
        k1_compute(lds, wid, lane, row0, type, grp, x, ws);
    }
}

constexpr int AT_CH = 4 * 3072;
constexpr int AT_STAGE = 2 * AT_CH, AT_NST = 3, AT_LDS = AT_STAGE * AT_NST;

__device__ __forceinline__ f32x16 attn_fallback(const char* kvh, const char* q2, int lane) {
    const int r32 = lane & 31, h = lane >> 5;
    const bf16x8 qa = *(const bf16x8*)(q2 + lane * 16); bf16x4 qb = *(const bf16x4*)(q2 + 1024 + r32 * 8);
    if (h) qb = (bf16x4){0, 0, 0, 0};
    f32x16 o = f32x16{}; float m = -INFINITY;
    const int vrow = r32 < 20 ? r32 : 20;
#pragma unroll 1
    for (int t = 0; t < 64; ++t) {
        const char* kt = kvh + (size_t)t * KVT_B;
        const bf16x8 ka = *(const bf16x8*)(kt + lane * 16); const bf16x4 kb = *(const bf16x4*)(kt + 1024 + lane * 8);
        const bf16x8 v0 = *(const bf16x8*)(kt + 1536 + (h * 24 + vrow) * 16), v1 = *(const bf16x8*)(kt + 1536 + 768 + (h * 24 + vrow) * 16);
        f32x16 s = MFMA16(ka, qa, f32x16{}); s = MFMA8(kb, qb, s);
        float tm = s[0];
#pragma unroll
        for (int i = 1; i < 16; ++i) tm = __builtin_fmaxf(tm, s[i]);
        tm = xhalf_max(tm);
        const float mn = __builtin_fmaxf(m, tm);
        const float al = m == mn ? 1.0f : EX2(m - mn);
        m = mn;
#pragma unroll
        for (int i = 0; i < 16; ++i) { o[i] *= al; s[i] = EX2(s[i] - mn); }
        const u32x4 p0 = {pk2(s[0], s[1]), pk2(s[2], s[3]), pk2(s[4], s[5]), pk2(s[6], s[7])}, p1 = {pk2(s[8], s[9]), pk2(s[10], s[11]), pk2(s[12], s[13]), pk2(s[14], s[15])};
        o = MFMA16(v0, __builtin_bit_cast(bf16x8, p0), o); o = MFMA16(v1, __builtin_bit_cast(bf16x8, p1), o);
    }
    return o;
}

__device__ __forceinline__ void attn_block(lds_ptr lds, int bid, char* ws) {
    const int lane = threadIdx.x & 63, wid = RFL(threadIdx.x >> 6);
    const int r32 = lane & 31, h = lane >> 5;
    const int head = (bid & 7) * 4 + (bid >> 6), qblk = (bid >> 3) & 7;
    const int qg = wid & 3, kh = wid >> 2;
    const char* kvh = ws + WS_KV + (size_t)head * 64 * KVT_B;
    const unsigned l0 = (unsigned)(uintptr_t)lds;
    const char* src = kvh + (size_t)kh * 32 * KVT_B + qg * 3072;
    const unsigned dst = l0 + kh * AT_CH + qg * 3072, lo16 = lane * 16;
    auto issue = [&](int c, int slot) {
#pragma unroll
        for (int i = 0; i < 3; ++i) glds16(lo16, src + (size_t)c * AT_CH + i * 1024, dst + slot * AT_STAGE + i * 1024);
    };
    const int qt0 = qblk * 8 + qg * 2;
    const char* qp = ws + WS_Q + (size_t)(head * 64 + qt0) * QT_B;
    bf16x8 qa0 = *(const bf16x8*)(qp + lane * 16), qa1 = *(const bf16x8*)(qp + QT_B + lane * 16);
    bf16x4 qb0 = *(const bf16x4*)(qp + 1024 + r32 * 8), qb1 = *(const bf16x4*)(qp + QT_B + 1024 + r32 * 8);
    { const bf16x8 ka = *(const bf16x8*)(kvh + lane * 16); const bf16x4 kb = *(const bf16x4*)(kvh + 1024 + lane * 8);
      issue(0, 0); issue(1, 1);
      if (h) { qb0 = (bf16x4){0, 0, 0, 0}; qb1 = (bf16x4){0, 0, 0, 0}; }
      f32x16 s0 = MFMA16(ka, qa0, f32x16{}); s0 = MFMA8(kb, qb0, s0);
      f32x16 s1 = MFMA16(ka, qa1, f32x16{}); s1 = MFMA8(kb, qb1, s1);
      float m0 = s0[0], m1 = s1[0];
#pragma unroll
      for (int i = 1; i < 16; ++i) { m0 = __builtin_fmaxf(m0, s0[i]); m1 = __builtin_fmaxf(m1, s1[i]); }
      m0 = xhalf_max(m0); m1 = xhalf_max(m1);
      if (h) { qb0[0] = (short)(pk2(-m0, 0.f) & 0xffffu); qb1[0] = (short)(pk2(-m1, 0.f) & 0xffffu); } }
    const int vrow = r32 < 20 ? r32 : 20;
    const lds_ptr kap = lds + kh * AT_CH + lane * 16, kbp = lds + kh * AT_CH + 1024 + lane * 8, vp = lds + kh * AT_CH + 1536 + (h * 24 + vrow) * 16;
    f32x16 o0 = f32x16{}, o1 = f32x16{}, s0, s1;
    u32x4 p0a, p0b, p1a = {0, 0, 0, 0}, p1b = {0, 0, 0, 0};
    bf16x8 ka, va = {0, 0, 0, 0, 0, 0, 0, 0}, vb = {0, 0, 0, 0, 0, 0, 0, 0}; bf16x4 kb;
    WAITV_BAR(3);
    ka = lds_r128(kap); kb = lds_r64(kbp);
    s0 = MFMA16(ka, qa0, f32x16{}); s0 = MFMA8(kb, qb0, s0);
#define G4(MF, X, i, P, j) do { MF; X[i] = EX2(X[i]); X[i + 1] = EX2(X[i + 1]); X[i + 2] = EX2(X[i + 2]); X[i + 3] = EX2(X[i + 3]); \
        P[j] = pk2(X[i], X[i + 1]); P[j + 1] = pk2(X[i + 2], X[i + 3]); PIN(P); SBAR(); } while (0)
    int slot = 0;
#pragma unroll 1
    for (int c = 0; c < 8; ++c) {
        const int sb = slot * AT_STAGE; const int nslot = slot == 2 ? 0 : slot + 1;
#pragma unroll
        for (int tt = 0; tt < 4; ++tt) {
            G4(o1 = MFMA16(va, __builtin_bit_cast(bf16x8, p1a), o1), s0, 0, p0a, 0);
            G4(o1 = MFMA16(vb, __builtin_bit_cast(bf16x8, p1b), o1), s0, 4, p0a, 2);
            va = lds_r128(vp + sb + tt * 3072); vb = lds_r128(vp + sb + tt * 3072 + 768);
            G4(s1 = MFMA16(ka, qa1, f32x16{}), s0, 8, p0b, 0);
            G4(s1 = MFMA8(kb, qb1, s1), s0, 12, p0b, 2);
            if (tt == 3) {
                if (c < 7) { WAITV_BAR(0);
                    if (c + 2 < 8) issue(c + 2, nslot == 2 ? 0 : nslot + 1);
                    ka = lds_r128(kap + nslot * AT_STAGE); kb = lds_r64(kbp + nslot * AT_STAGE); }
            } else { ka = lds_r128(kap + sb + (tt + 1) * 3072); kb = lds_r64(kbp + sb + (tt + 1) * 3072); }
            G4(o0 = MFMA16(va, __builtin_bit_cast(bf16x8, p0a), o0), s1, 0, p1a, 0);
            G4(o0 = MFMA16(vb, __builtin_bit_cast(bf16x8, p0b), o0), s1, 4, p1a, 2);
            G4(s0 = MFMA16(ka, qa0, f32x16{}), s1, 8, p1b, 0);
            G4(s0 = MFMA8(kb, qb0, s0), s1, 12, p1b, 2);
        }
        slot = nslot;
    }
    o1 = MFMA16(va, __builtin_bit_cast(bf16x8, p1a), o1); o1 = MFMA16(vb, __builtin_bit_cast(bf16x8, p1b), o1);
#undef G4
    BAR_LGKM();
    LAS f32x4* ex = (LAS f32x4*)lds + (wid * 3) * 64 + lane;
    { const f32x16 give = kh ? o0 : o1;
#pragma unroll
      for (int g = 0; g < 3; ++g) ex[g * 64] = (f32x4){give[4 * g], give[4 * g + 1], give[4 * g + 2], give[4 * g + 3]}; }
    BAR_LGKM();
    f32x16 o = kh ? o1 : o0;
    { const LAS f32x4* ey = (const LAS f32x4*)lds + ((wid ^ 4) * 3) * 64 + lane;
#pragma unroll
      for (int g = 0; g < 3; ++g) { const f32x4 a = ey[g * 64];
#pragma unroll
        for (int e = 0; e < 4; ++e) o[4 * g + e] += a[e]; } }
    float l = xhalf_sum(h ? o[8] : 0.f);
    float chk = 0.f;
#pragma unroll
    for (int i = 0; i < 12; ++i) chk = __builtin_fmaf(o[i], 0.f, chk);
    const bool bad = !(l > 8.6736174e-19f && l < 1.1529215e18f && chk == 0.f);
    if (__builtin_expect(__any(bad), 0)) {
        o = attn_fallback(kvh, qp + kh * QT_B, lane);
        l = xhalf_sum(h ? o[8] : 0.f);
    }
    const float rl = RCP(l);
    char* ob = ws + WS_O + (size_t)((qt0 + kh) * 40 + 5 * (head >> 2)) * 1024;
    *(u32x4*)(ob + (head & 3) * 1024 + lane * 16) = (u32x4){pk2(o[0] * rl, o[1] * rl), pk2(o[2] * rl, o[3] * rl), pk2(o[4] * rl, o[5] * rl), pk2(o[6] * rl, o[7] * rl)};
    if (!h) *(u32x2*)(ob + 4096 + ((head >> 1) & 1) * 512 + r32 * 16 + (head & 1) * 8) = (u32x2){pk2(o[8] * rl, o[9] * rl), pk2(o[10] * rl, o[11] * rl)};
}

#define GLD(dst, voff, sbase, imm) asm volatile("global_load_dwordx4 %0, %1, %2 offset:" #imm : "=v"(dst) : "v"(voff), "s"(sbase) : "memory")
template <int NT>
__device__ __forceinline__ void mlp2_wave(lds_ptr red, int kq, int lane, const char* xs, const char* wsrc, const unsigned short* b2f, unsigned short* gout) {
    const int r32 = lane & 31, h = lane >> 5;
    unsigned vo[NT];
#pragma unroll
    for (int i = 0; i < NT; ++i) vo[i] = lane * 16 + i * 108 * 1024;
    f32x16 acc[NT];
#pragma unroll
    for (int i = 0; i < NT; ++i) acc[i] = f32x16{};
    u32x4 xf[3], wf[3][NT];
#define LD_STEP(r, imm) do { GLD(xf[r], vo[0], xs, imm); _Pragma("unroll") for (int i = 0; i < NT; ++i) GLD(wf[r][i], vo[i], wsrc, imm); } while (0)
#define USE_STEP(r) do { PIN(xf[r]); _Pragma("unroll") for (int i = 0; i < NT; ++i) { PIN(wf[r][i]); \
        acc[i] = MFMA16(__builtin_bit_cast(bf16x8, wf[r][i]), __builtin_bit_cast(bf16x8, xf[r]), acc[i]); } SBAR(); } while (0)
#define WAITN(n) asm volatile("s_waitcnt vmcnt(%0)" :: "i"(n) : "memory")
    LD_STEP(0, 0); LD_STEP(1, 1024); LD_STEP(2, 2048);
#pragma unroll 1
    for (int it = 0; it < 8; ++it) {
        xs += 3072; wsrc += 3072;
        WAITN(2 * (NT + 1)); USE_STEP(0); LD_STEP(0, 0);
        WAITN(2 * (NT + 1)); USE_STEP(1); LD_STEP(1, 1024);
        WAITN(2 * (NT + 1)); USE_STEP(2); LD_STEP(2, 2048);
    }
    WAITN(2 * (NT + 1)); USE_STEP(0); WAITN(NT + 1); USE_STEP(1); WAITN(0); USE_STEP(2);
    LAS f32x4* rb = (LAS f32x4*)red;
#pragma unroll
    for (int d = 1; d < 4; ++d) { const int dq = (kq + d) & 3;
#pragma unroll
        for (int i = 0; i < NT; ++i) {
            const f32x4 v = dq == 0 ? (f32x4){acc[i][0], acc[i][1], acc[i][2], acc[i][3]} : dq == 1 ? (f32x4){acc[i][4], acc[i][5], acc[i][6], acc[i][7]}
                          : dq == 2 ? (f32x4){acc[i][8], acc[i][9], acc[i][10], acc[i][11]} : (f32x4){acc[i][12], acc[i][13], acc[i][14], acc[i][15]};
            rb[((dq * 3 + (d - 1)) * NT + i) * 64 + lane] = v; } }
    BAR_LGKM();
#pragma unroll
    for (int i = 0; i < NT; ++i) {
        f32x4 v = kq == 0 ? (f32x4){acc[i][0], acc[i][1], acc[i][2], acc[i][3]} : kq == 1 ? (f32x4){acc[i][4], acc[i][5], acc[i][6], acc[i][7]}
                : kq == 2 ? (f32x4){acc[i][8], acc[i][9], acc[i][10], acc[i][11]} : (f32x4){acc[i][12], acc[i][13], acc[i][14], acc[i][15]};
#pragma unroll
        for (int d = 0; d < 3; ++d) v += rb[((kq * 3 + d) * NT + i) * 64 + lane];
        const int f = 32 * i + 8 * kq + 4 * h;
        const u32x2 bb = *(const u32x2*)(b2f + f);
        const float u0 = v[0] + bf_lo(bb[0]), u1 = v[1] + bf_hi(bb[0]), u2 = v[2] + bf_lo(bb[1]), u3 = v[3] + bf_hi(bb[1]);
#define SILU(u) ((u) * RCP(1.0f + EX2((u) * -LOG2E)))
        *(u32x2*)(gout + r32 * D_ + f) = (u32x2){pk2(SILU(u0), SILU(u1)), pk2(SILU(u2), SILU(u3))};
    }
}
__device__ __forceinline__ void mlp2_block(lds_ptr lds, int bid, const unsigned short* b2, char* ws) {
    const int lane = threadIdx.x & 63, wid = RFL(threadIdx.x >> 6), kq = wid & 3;
    const int tt = (bid & 7) * 8 + ((bid >> 3) & 7), cg = bid >> 6;
    const char* xs = ws + WS_H + (size_t)(tt * 108 + 27 * kq) * 1024;
    const int ft = 5 * cg + (wid < 4 ? 0 : 3);
    const char* wsrc = ws + WS_W2 + (size_t)(ft * 108 + 27 * kq) * 1024;
    unsigned short* gout = (unsigned short*)(ws + WS_G) + (size_t)tt * 32 * D_ + ft * 32;
    if (wid < 4) mlp2_wave<3>(lds, kq, lane, xs, wsrc, b2 + ft * 32, gout);
    else mlp2_wave<2>(lds + 36864, kq, lane, xs, wsrc, b2 + ft * 32, gout);
}

__global__ __launch_bounds__(512, 4) void k2_attn_mlp2(const unsigned short* b2, char* ws, int mode) {
    extern __shared__ __attribute__((aligned(16))) char smem[];
    const int b = blockIdx.x + mode;
    if (b < 256) attn_block((lds_ptr)smem, b, ws); else mlp2_block((lds_ptr)smem, b - 256, b2, ws);
}

constexpr int K3_ROW = 656, K3_PART = 32 * K3_ROW, K3_LDS = 4 * K3_PART;
template <int NT>
__device__ __forceinline__ void wo_wave(lds_ptr part, int lane, const char* xs, const char* wsrc) {
    const int r32 = lane & 31, h = lane >> 5;
    bf16x8 xf[10], wf[10][NT];
#pragma unroll
    for (int k = 0; k < 10; ++k) { xf[k] = *(const bf16x8*)(xs + k * 1024 + lane * 16);
#pragma unroll
        for (int i = 0; i < NT; ++i) wf[k][i] = *(const bf16x8*)(wsrc + (i * 40 + k) * 1024 + lane * 16); }
    f32x16 acc[NT];
#pragma unroll
    for (int i = 0; i < NT; ++i) acc[i] = f32x16{};
#pragma unroll
    for (int k = 0; k < 10; ++k)
#pragma unroll
        for (int i = 0; i < NT; ++i) acc[i] = MFMA16(wf[k][i], xf[k], acc[i]);
#pragma unroll
    for (int i = 0; i < NT; ++i)
#pragma unroll
        for (int g = 0; g < 4; ++g) *(LAS f32x4*)(part + r32 * K3_ROW + (32 * i + 8 * g + 4 * h) * 4) = (f32x4){acc[i][4 * g], acc[i][4 * g + 1], acc[i][4 * g + 2], acc[i][4 * g + 3]};
}
__global__ __launch_bounds__(512, 2) void k3_out(const unsigned short* x, const char* ws, unsigned short* out) {
    extern __shared__ __attribute__((aligned(16))) char smem[];
    const lds_ptr lds = (lds_ptr)smem;
    const int lane = threadIdx.x & 63, wid = RFL(threadIdx.x >> 6), kq = wid & 3, bid = blockIdx.x;
    const int tt = (bid & 7) * 8 + ((bid >> 3) & 7), cg = bid >> 6;
    const int t = threadIdx.x, tok = (t * 6554) >> 16, c = t - tok * 10;
    const size_t eo = (size_t)(tt * 32 + tok) * D_ + cg * 160 + c * 16;
    u32x4 xv[2], gv[2];
    if (t < 320) { const unsigned short* g = (const unsigned short*)(ws + WS_G);
        xv[0] = *(const u32x4*)(x + eo); xv[1] = *(const u32x4*)(x + eo + 8); gv[0] = *(const u32x4*)(g + eo); gv[1] = *(const u32x4*)(g + eo + 8); }
    const char* xs = ws + WS_O + (size_t)(tt * 40 + 10 * kq) * 1024;
    const int ft = 5 * cg + (wid < 4 ? 0 : 3);
    const char* wsrc = ws + WS_WO + (size_t)(ft * 40 + 10 * kq) * 1024;
    if (wid < 4) wo_wave<3>(lds + kq * K3_PART, lane, xs, wsrc); else wo_wave<2>(lds + kq * K3_PART + 384, lane, xs, wsrc);
    __syncthreads();
    if (t < 320) {
#pragma unroll
        for (int j = 0; j < 2; ++j) {
            u32x4 o;
#pragma unroll
            for (int e = 0; e < 2; ++e) {
                f32x4 s = *(const LAS f32x4*)(lds + tok * K3_ROW + c * 64 + j * 32 + e * 16);
#pragma unroll
                for (int p = 1; p < 4; ++p) s += *(const LAS f32x4*)(lds + p * K3_PART + tok * K3_ROW + c * 64 + j * 32 + e * 16);
                o[2 * e] = pk2(__builtin_fmaf(s[0], bf_lo(gv[j][2 * e]), bf_lo(xv[j][2 * e])), __builtin_fmaf(s[1], bf_hi(gv[j][2 * e]), bf_hi(xv[j][2 * e])));
                o[2 * e + 1] = pk2(__builtin_fmaf(s[2], bf_lo(gv[j][2 * e + 1]), bf_lo(xv[j][2 * e + 1])), __builtin_fmaf(s[3], bf_hi(gv[j][2 * e + 1]), bf_hi(xv[j][2 * e + 1])));
            }
            *(u32x4*)(out + eo + j * 8) = o;
        }
    }
}

extern "C" void kernel_launch(void* const* d_in, const int* in_sizes, int n_in, void* d_out, int out_size, void* d_ws, size_t ws_size, hipStream_t stream) {
    const unsigned short* x = (const unsigned short*)d_in[0]; const unsigned short* wqkv = (const unsigned short*)d_in[1];
    const unsigned short* wo = (const unsigned short*)d_in[2]; const unsigned short* w1 = (const unsigned short*)d_in[4];
    const unsigned short* b1 = (const unsigned short*)d_in[5]; const unsigned short* w2 = (const unsigned short*)d_in[6];
    const unsigned short* b2 = (const unsigned short*)d_in[7];
    char* ws = (char*)d_ws;
    (void)hipFuncSetAttribute((const void*)k1_proj, hipFuncAttributeMaxDynamicSharedMemorySize, K1_LDS);
    (void)hipFuncSetAttribute((const void*)k2_attn_mlp2, hipFuncAttributeMaxDynamicSharedMemorySize, AT_LDS);
    (void)hipFuncSetAttribute((const void*)k3_out, hipFuncAttributeMaxDynamicSharedMemorySize, K3_LDS);
    k1_proj<<<256, 768, K1_LDS, stream>>>(x, wqkv, w1, b1, w2, wo, ws);
    k2_attn_mlp2<<<512, 512, AT_LDS, stream>>>(b2, ws, 0);
    k3_out<<<256, 512, K3_LDS, stream>>>(x, ws, (unsigned short*)d_out);
}
```

```cpp
#include <hip/hip_runtime.h>
#include <hip/hip_bf16.h>
#include <stdint.h>

typedef short bf16x8 __attribute__((ext_vector_type(8)));
typedef short bf16x4 __attribute__((ext_vector_type(4)));
typedef float f32x16 __attribute__((ext_vector_type(16)));
typedef float f32x4 __attribute__((ext_vector_type(4)));
typedef float f32x2 __attribute__((ext_vector_type(2)));
typedef unsigned u32x4 __attribute__((ext_vector_type(4)));
typedef unsigned u32x2 __attribute__((ext_vector_type(2)));
typedef __bf16 bf16x2_t __attribute__((ext_vector_type(2)));
typedef short v4i16_t __attribute__((ext_vector_type(4)));
#define LAS __attribute__((address_space(3)))
typedef LAS char* lds_ptr;

constexpr int S_ = 2048, D_ = 640, F_ = 1728, NH = 32;
constexpr float LN_EPS = 1e-5f;
constexpr float LOG2E = 1.4426950408889634f;
constexpr float QSCALE = 0.22360679774997896f * LOG2E;

constexpr size_t QT_B = 1280, KVT_B = 3072;
constexpr size_t WS_Q = 0;
constexpr size_t WS_KV = WS_Q + (size_t)NH * 64 * QT_B;
constexpr size_t WS_H = WS_KV + (size_t)NH * 64 * KVT_B;
constexpr size_t WS_O = WS_H + (size_t)64 * 108 * 1024;
constexpr size_t WS_G = WS_O + (size_t)64 * 40 * 1024;
constexpr size_t WS_W2 = WS_G + (size_t)S_ * D_ * 2;
constexpr size_t WS_WO = WS_W2 + (size_t)20 * 108 * 1024;
constexpr size_t WS_END = WS_WO + (size_t)20 * 40 * 1024;

#define TS(k, slot) do {} while (0)
#define MFMA16(a, b, c) __builtin_amdgcn_mfma_f32_32x32x16_bf16(a, b, c, 0, 0, 0)
#define MFMA8(a, b, c) __builtin_amdgcn_mfma_f32_32x32x8bf16_1k(a, b, c, 0, 0, 0)
#define SBAR() __builtin_amdgcn_sched_barrier(0)
#define PIN(x) asm volatile("" : "+v"(x))
#define RFL(x) __builtin_amdgcn_readfirstlane(x)

__device__ __forceinline__ const char* uni(const void* p) {
    const unsigned long long a = (unsigned long long)p; return (const char*)(((unsigned long long)(unsigned)RFL((unsigned)(a >> 32)) << 32) | (unsigned)RFL((unsigned)a)); }
__device__ __forceinline__ unsigned pk2(float a, float b) { f32x2 v = {a, b}; return __builtin_bit_cast(unsigned, __builtin_convertvector(v, bf16x2_t)); }
__device__ __forceinline__ unsigned pk2v(f32x2 v) { return __builtin_bit_cast(unsigned, __builtin_convertvector(v, bf16x2_t)); }
__device__ __forceinline__ float bf_lo(unsigned u) { return __uint_as_float(u << 16); }
__device__ __forceinline__ float bf_hi(unsigned u) { return __uint_as_float(u & 0xffff0000u); }
__device__ __forceinline__ float xhalf_sum(float v) {
    auto r = __builtin_amdgcn_permlane32_swap(__float_as_uint(v), __float_as_uint(v), false, false);
    return __uint_as_float(r[0]) + __uint_as_float(r[1]); }
__device__ __forceinline__ float xhalf_max(float v) {
    auto r = __builtin_amdgcn_permlane32_swap(__float_as_uint(v), __float_as_uint(v), false, false);
    return __builtin_fmaxf(__uint_as_float(r[0]), __uint_as_float(r[1])); }
__device__ __forceinline__ void glds16(unsigned voff, const void* sbase, unsigned lds_base) {
    unsigned sv; asm volatile("s_mov_b32 %0, m0\n\ts_mov_b32 m0, %3\n\ts_nop 0\n\tglobal_load_lds_dwordx4 %1, %2\n\ts_mov_b32 m0, %0"
                              : "=&s"(sv) : "v"(voff), "s"(sbase), "s"(lds_base) : "memory"); }
__device__ __forceinline__ bf16x8 lds_r128(lds_ptr p) { return *(const LAS bf16x8*)p; }
__device__ __forceinline__ bf16x4 lds_r64(lds_ptr p) { return *(const LAS bf16x4*)p; }
__device__ __forceinline__ bf16x4 lds_tr(lds_ptr p) { return __builtin_bit_cast(bf16x4, __builtin_amdgcn_ds_read_tr16_b64_v4i16((LAS v4i16_t*)p)); }
__device__ __forceinline__ bf16x8 cat8(bf16x4 a, bf16x4 b) { return (bf16x8){a[0], a[1], a[2], a[3], b[0], b[1], b[2], b[3]}; }
#define WAITV_BAR(N) asm volatile("s_waitcnt vmcnt(" #N ") lgkmcnt(0)\n\ts_barrier" ::: "memory")
#define BAR_LGKM() asm volatile("s_waitcnt lgkmcnt(0)\n\ts_barrier" ::: "memory")
#define EX2(v) __builtin_amdgcn_exp2f(v)
#define RCP(v) __builtin_amdgcn_rcpf(v)

constexpr int K1_STAGE = 49152, K1_NST = 3, K1_STATS = K1_STAGE * K1_NST, K1_LDS = K1_STATS + 4 * 1024 + 4 * 512 + 256;
constexpr int K1_GEMM_BLOCKS = 232;
#define K1_ROT 0

#define TOUCH 0
#define STATS_MODE 0
#define ST16(p, v) asm volatile("global_store_dwordx4 %0, %1, off sc1\n\ts_nop 1" :: "v"(p), "v"(v) : "memory")
__device__ __forceinline__ void acc_sum(float& s, float v) { asm("v_add_f32 %0, %0, %1" : "+v"(s) : "v"(v)); }
__device__ __forceinline__ void acc_sq(float& s, float v) { asm("v_fmac_f32 %0, %1, %1" : "+v"(s) : "v"(v)); }
__device__ __forceinline__ float dot2(unsigned a, unsigned b, float c) {
    return __builtin_amdgcn_fdot2_f32_bf16(__builtin_bit_cast(bf16x2_t, a), __builtin_bit_cast(bf16x2_t, b), c, false); }

template <bool V0, bool V1>
__device__ __forceinline__ void k1_main(lds_ptr lds, int wf, int wt, int lane, f32x16 (&acc)[2][2], float& sx, float& sxx, float& cs0, float& cs1, const char* touch) {
    const int r32 = lane & 31, h = lane >> 5;
    unsigned xo[4][2], wa[4];
    { const int blk = (lane >> 4) & 1, q = (lane & 15) >> 2, p = lane & 3, ch = 4 * wf + 2 * blk + (p >> 1);
      const unsigned l0 = (unsigned)(uintptr_t)lds, a = l0 + 32768 + (8 * h + q) * 128 + 16 * (ch ^ ((q >> 1) << 2)) + 8 * (p & 1);
#pragma unroll
      for (int ks = 0; ks < 4; ++ks) { wa[ks] = a + (ks ^ wt) * 2048;
#pragma unroll
          for (int jp = 0; jp < 2; ++jp) { const int row = 64 * wt + 32 * (jp ^ wf) + r32; xo[ks][jp] = l0 + row * 128 + (((2 * (ks ^ wt) + h) ^ ((row >> 1) & 7)) << 4); } } }
#pragma unroll
    for (int a = 0; a < 2; ++a)
#pragma unroll
        for (int b = 0; b < 2; ++b) acc[a][b] = f32x16{};
    float sa = 0.f, sb = 0.f, qa = 0.f, qb = 0.f, c0 = 0.f, c1 = 0.f;
    bf16x8 fx0[2], fx1[2], fw0[2], fw1[2];
#define LP(a) ((lds_ptr)(uintptr_t)(a))
#define K1_LD(B, ks) do { fx0[B] = lds_r128(LP(xo[ks][0])); fw0[B] = cat8(lds_tr(LP(wa[ks])), lds_tr(LP(wa[ks]) + 512)); \
        fx1[B] = lds_r128(LP(xo[ks][1])); fw1[B] = cat8(lds_tr(LP(wa[ks]) + 8192), lds_tr(LP(wa[ks]) + 8192 + 512)); SBAR(); } while (0)
#define XD 0
#define WD 1
#define K1_STX(B, j) do { const unsigned u_ = __builtin_bit_cast(u32x4, fx0[B])[j]; const float lo_ = bf_lo(u_), hi_ = bf_hi(u_); \
        acc_sum(sa, lo_); acc_sum(sb, hi_); acc_sq(qa, lo_); acc_sq(qb, hi_); } while (0)
#define K1_STW(B, j, ks) do { const unsigned w_ = __builtin_bit_cast(u32x4, ks ? fw1[B] : fw0[B])[j]; acc_sum(ks ? c1 : c0, bf_lo(w_)); acc_sum(ks ? c1 : c0, bf_hi(w_)); } while (0)
#define K1_ST(B, j, ks) do { if (3 - j >= XD) K1_STX(B, 3 - j); if (!WD && ks < 2) K1_STW(B, j, ks); } while (0)
#define K1_DOTS(B, ks) do { _Pragma("unroll") for (int j_ = 0; j_ < XD; ++j_) { const unsigned u_ = __builtin_bit_cast(u32x4, fx0[B])[j_]; sd = dot2(u_, 0x3F803F80u, sd); qd = dot2(u_, u_, qd); } \
        if (WD && ks < 2) { _Pragma("unroll") for (int j_ = 0; j_ < 4; ++j_) { if (ks) c1 = dot2(__builtin_bit_cast(u32x4, fw1[B])[j_], 0x3F803F80u, c1); else c0 = dot2(__builtin_bit_cast(u32x4, fw0[B])[j_], 0x3F803F80u, c0); } } SBAR(); } while (0)
    float sd = 0.f, qd = 0.f;
#define K1_MM(B, ks) do { \
        acc[0][0] = V0 ? MFMA16(fx0[B], fw0[B], acc[0][0]) : MFMA16(fw0[B], fx0[B], acc[0][0]); K1_ST(B, 0, ks); SBAR(); \
        acc[0][1] = V0 ? MFMA16(fx1[B], fw0[B], acc[0][1]) : MFMA16(fw0[B], fx1[B], acc[0][1]); K1_ST(B, 1, ks); SBAR(); \
        acc[1][0] = V1 ? MFMA16(fx0[B], fw1[B], acc[1][0]) : MFMA16(fw1[B], fx0[B], acc[1][0]); K1_ST(B, 2, ks); SBAR(); \
        acc[1][1] = V1 ? MFMA16(fx1[B], fw1[B], acc[1][1]) : MFMA16(fw1[B], fx1[B], acc[1][1]); K1_ST(B, 3, ks); SBAR(); K1_DOTS(B, ks); } while (0)
    unsigned tdummy;
    BAR_LGKM();
    K1_LD(0, 0);
    int slot = 0;
#pragma unroll 1
    for (int s = 0; s < 10; ++s) {
        const int adv = slot == K1_NST - 1 ? -(K1_NST - 1) * K1_STAGE : K1_STAGE; slot = slot == K1_NST - 1 ? 0 : slot + 1;
        K1_LD(1, 1); xo[0][0] += adv; xo[0][1] += adv; wa[0] += adv; xo[1][0] += adv; xo[1][1] += adv; wa[1] += adv;
        K1_MM(0, 0);
        K1_LD(0, 2); xo[2][0] += adv; xo[2][1] += adv; wa[2] += adv;
        K1_MM(1, 1);
        K1_LD(1, 3); xo[3][0] += adv; xo[3][1] += adv; wa[3] += adv;
        K1_MM(0, 2);
        if (s < 9) { BAR_LGKM(); K1_LD(0, 0); }
        K1_MM(1, 3);
    }
#undef K1_LD
#undef K1_ST
#undef K1_MM
    sx = xhalf_sum((sa + sb) + sd); sxx = xhalf_sum((qa + qb) + qd); cs0 = xhalf_sum(c0); cs1 = xhalf_sum(c1);
}

__device__ __forceinline__ f32x4 k1_cs4(const LAS float* s_cs, int f) {
    return (*(const LAS f32x4*)(s_cs + f) + *(const LAS f32x4*)(s_cs + 128 + f)) + (*(const LAS f32x4*)(s_cs + 256 + f) + *(const LAS f32x4*)(s_cs + 384 + f)); }

__device__ __forceinline__ void k1_store_v(const f32x16 (&acc)[2], const LAS float* s_r, const LAS float* s_cs, int wf, int wt, int lane, int row0, int grp, char* ws) {
    const int r32 = lane & 31, h = lane >> 5;
    const int fl = 32 * wf + r32, f = 64 * (grp - 20) + fl, hd = (f * 3277) >> 16, d = f - hd * 20;
    const float c = -((s_cs[fl] + s_cs[128 + fl]) + (s_cs[256 + fl] + s_cs[384 + fl]));
#pragma unroll
    for (int jp = 0; jp < 2; ++jp) {
        const int tl = 64 * wt + 32 * (jp ^ wf);
        char* base = ws + WS_KV + (size_t)(hd * 64 + ((row0 + tl) >> 5)) * KVT_B + 1536 + (h * 24 + d) * 16;
#pragma unroll
        for (int s = 0; s < 2; ++s) {
            u32x4 o;
#pragma unroll
            for (int gg = 0; gg < 2; ++gg) { const int g = 2 * s + gg;
                const f32x4 r4 = *(const LAS f32x4*)(s_r + tl + 8 * g + 4 * h), m4 = *(const LAS f32x4*)(s_r + 256 + tl + 8 * g + 4 * h);
#pragma unroll
                for (int e = 0; e < 2; ++e) { const f32x2 a = {acc[jp][4 * g + 2 * e], acc[jp][4 * g + 2 * e + 1]}, r2 = {r4[2 * e], r4[2 * e + 1]}, m2 = {m4[2 * e], m4[2 * e + 1]};
                    o[2 * gg + e] = pk2v(__builtin_elementwise_fma(a, r2, m2 * c)); } }
            ST16(base + s * 768, o);
            if (d >= 18) { const unsigned k = d == 19 ? 0x3F803F80u : 0u; const u32x4 kk = {k, k, k, k};
                ST16(base + s * 768 + (d == 19 ? 16 : 48), kk); }
        }
    }
}

__device__ __forceinline__ void k1_compute(lds_ptr lds, int wid, int lane, int row0, int cb, int g0, int g1, const unsigned short* x, char* ws, const char* touch) {
    const int wf = wid >> 2, wt = wid & 3, r32 = lane & 31, h = lane >> 5;
    f32x16 acc[2][2]; float sx, sxx, cs0, cs1;
    if (cb < 20) k1_main<false, false>(lds, wf, wt, lane, acc, sx, sxx, cs0, cs1, touch);
    else if (cb < 27) k1_main<true, false>(lds, wf, wt, lane, acc, sx, sxx, cs0, cs1, touch);
    else k1_main<true, true>(lds, wf, wt, lane, acc, sx, sxx, cs0, cs1, touch);
    TS(0, 2);
    LAS float* s_r = (LAS float*)(lds + K1_STATS);
    LAS float* s_cs = s_r + 1024; LAS float* s_b1 = s_r + 1536;
    { const int tok = 64 * wt + 32 * wf + r32;
      const float m = sx * (1.0f / D_);
      float var = __builtin_fmaf(-m, m, sxx * (1.0f / D_));
      if (__builtin_expect(__any(!(m * m <= 64.0f * var)), 0)) {
          const unsigned short* row = x + (size_t)(row0 + tok) * D_; float a = 0.f;
#pragma unroll 1
          for (int c = 0; c < D_ / 8; ++c) { const u32x4 v = *(const u32x4*)(row + c * 8);
#pragma unroll
              for (int j = 0; j < 4; ++j) { const float lo = bf_lo(v[j]) - m, hi = bf_hi(v[j]) - m; a = __builtin_fmaf(lo, lo, a); a = __builtin_fmaf(hi, hi, a); } }
          var = a * (1.0f / D_);
      }
      const float r = __builtin_amdgcn_rsqf(var + LN_EPS);
      const float a2 = r * __builtin_amdgcn_rsqf(__builtin_fmaf(var * r, r, LN_EPS));
      if (h == 0) { s_r[tok] = r; s_r[256 + tok] = r * m; s_r[512 + tok] = a2; s_r[768 + tok] = a2 * m; }
      s_cs[wt * 128 + 64 * h + 32 * wf + r32] = h ? cs1 : cs0; }
    BAR_LGKM();
    TS(0, 3);
    if (cb < 20) {
        float rr[2], rm[2];
        const bool isq = cb < 10;
#pragma unroll
        for (int jp = 0; jp < 2; ++jp) { const int t = 64 * wt + 32 * (jp ^ wf) + r32; const float sc = isq ? QSCALE : 1.0f; rr[jp] = s_r[t] * sc; rm[jp] = -s_r[256 + t] * sc; }
        char* base = isq ? ws + WS_Q : ws + WS_KV; const int tb = isq ? (int)QT_B : (int)KVT_B;
#pragma unroll
        for (int g = 0; g < 4; ++g) {
            const int fl = 32 * wf + 8 * g + 4 * h, f0 = 64 * (isq ? cb : cb - 10) + fl;
            const f32x4 c4 = k1_cs4(s_cs, fl);
            const int hd = (f0 * 3277) >> 16, d0 = f0 - hd * 20;
            const int off = hd * 64 * tb + (d0 < 16 ? (d0 >> 3) * 512 + r32 * 16 + (d0 & 4) * 2 : 1024 + r32 * 8);
#pragma unroll
            for (int jp = 0; jp < 2; ++jp) {
                const int tile = (row0 >> 5) + 2 * wt + (jp ^ wf);
                u32x2 o;
#pragma unroll
                for (int e = 0; e < 2; ++e) { const f32x2 a = {acc[0][jp][4 * g + 2 * e], acc[0][jp][4 * g + 2 * e + 1]}, c2 = {c4[2 * e], c4[2 * e + 1]};
                    o[e] = pk2v(__builtin_elementwise_fma(a, (f32x2){rr[jp], rr[jp]}, c2 * rm[jp])); }
                char* p = base + off + tile * tb;
                *(u32x2*)p = o;
                if (!isq && d0 == 16) *(u32x2*)(p + 256) = (u32x2){0x00003F80u, 0u};
            }
        }
    } else k1_store_v(acc[0], s_r, s_cs, wf, wt, lane, row0, g0, ws);
    TS(0, 1);
    if (cb < 27) {
        constexpr float C1 = -2.0f * LOG2E * 0.7978845608028654f, C2 = C1 * 0.044715f;
        float a2[2], m2[2];
#pragma unroll
        for (int jp = 0; jp < 2; ++jp) { const int t = 64 * wt + 32 * (jp ^ wf) + r32; a2[jp] = s_r[512 + t]; m2[jp] = -s_r[768 + t]; }
#pragma unroll
        for (int s = 0; s < 2; ++s) {
            u32x4 o[2];
#pragma unroll
            for (int gg = 0; gg < 2; ++gg) {
                const int g = 2 * s + gg, fl = 32 * wf + 8 * g + 4 * h;
                const f32x4 c4 = k1_cs4(s_cs, 64 + fl), b4 = *(const LAS f32x4*)(s_b1 + fl);
#pragma unroll
                for (int jp = 0; jp < 2; ++jp) {
#pragma unroll
                    for (int e = 0; e < 2; ++e) {
                        const f32x2 a = {acc[1][jp][4 * g + 2 * e], acc[1][jp][4 * g + 2 * e + 1]}, c2 = {c4[2 * e], c4[2 * e + 1]}, bb = {b4[2 * e], b4[2 * e + 1]};
                        const f32x2 u = __builtin_elementwise_fma(a, (f32x2){a2[jp], a2[jp]}, __builtin_elementwise_fma(c2, (f32x2){m2[jp], m2[jp]}, bb));
                        const f32x2 w = u * __builtin_elementwise_fma(u * u, (f32x2){C2, C2}, (f32x2){C1, C1});
                        const f32x2 d = (f32x2){EX2(w[0]), EX2(w[1])} + 1.0f;
                        o[jp][2 * gg + e] = pk2v(u * (f32x2){RCP(d[0]), RCP(d[1])});
                    }
                }
            }
#pragma unroll
            for (int jp = 0; jp < 2; ++jp) {
                const int tile = (row0 >> 5) + 2 * wt + (jp ^ wf);
                ST16(ws + WS_H + (size_t)(tile * 108 + 4 * g1 + 2 * wf + s) * 1024 + lane * 16, o[jp]);
            }
        }
    } else if (cb == 27) k1_store_v(acc[1], s_r, s_cs + 64, wf, wt, lane, row0, g1, ws);
}

__device__ __forceinline__ void k1_loader(lds_ptr lds, int lw, int lane, int row0, const unsigned short* x, const char* w0, int ld0, const char* w1, int ld1,
                                          const unsigned short* b1g, int rot, char* ws) {
    const unsigned l0 = (unsigned)(uintptr_t)lds;
    unsigned xv[2];
#pragma unroll
    for (int pp = 0; pp < 2; ++pp) { const int row = 8 * pp + (lane >> 3); xv[pp] = row * (D_ * 2) + (((lane & 7) ^ ((row >> 1) & 7)) << 4); }
    const int wc = ((lane & 7) ^ (((lane >> 4) & 1) << 2)) << 4;
    const char* wsrc = lw < 2 ? w0 : w1; const int ldw = lw < 2 ? ld0 : ld1;
    const unsigned wv = (lane >> 3) * ldw + wc;
    wsrc += (size_t)(lw & 1) * 32 * ldw;
    const char* xsrc = (const char*)x + (size_t)(row0 + 64 * lw) * (D_ * 2);
    auto issue = [&](int s, int slot) {
        const unsigned base = l0 + slot * K1_STAGE;
        s += rot; if (s >= 10) s -= 10;
#pragma unroll
        for (int i = 0; i < 4; ++i) {
            glds16(wv, wsrc + (size_t)(s * 64 + 8 * i) * ldw, base + 32768 + (4 * lw + i) * 1024);
            glds16(xv[0], xsrc + i * 16 * (D_ * 2) + s * 128, base + (8 * lw + 2 * i) * 1024);
            glds16(xv[1], xsrc + i * 16 * (D_ * 2) + s * 128, base + (8 * lw + 2 * i + 1) * 1024);
        }
    };
    issue(0, 0);
    unsigned bv; asm volatile("global_load_ushort %0, %1, %2" : "=v"(bv) : "v"(lane * 2), "s"(b1g) : "memory");
    issue(1, 1);
    int slot = 2;
#pragma unroll 1
    for (int s = 0; s < 10; ++s) {
        if (s < 9) WAITV_BAR(12); else WAITV_BAR(0);
        if (s + 2 < 10) issue(s + 2, slot);
        if (s == 0) TS(0, 4);
        if (s == 1) TS(0, 5);
        if (s == 4) TS(0, 6);
        slot = slot == K1_NST - 1 ? 0 : slot + 1;
    }
    PIN(bv);
    if (lw == 0) ((LAS float*)(lds + K1_STATS))[1536 + lane] = __uint_as_float(bv << 16);
    BAR_LGKM();
}

__device__ __forceinline__ void k1_retile(lds_ptr lds, int gw, int wid, int lane, const unsigned short* w2, const unsigned short* wo, char* ws) {
    if (gw >= 260) return;
    const lds_ptr my = lds + wid * 12288; const unsigned l0 = (unsigned)(uintptr_t)my;
    const int h = lane >> 5, blk = (lane >> 4) & 1, q = (lane & 15) >> 2, p = lane & 3;
    const unsigned voff = (lane >> 2) * (D_ * 2) + (lane & 3) * 16;
    const unsigned ro = q * 64 + blk * 32 + p * 8;
    if (gw < 180) {
#pragma unroll
        for (int u = 0; u < 3; ++u) { const int un = 3 * gw + u, kb = un / 20, ft = un - kb * 20;
#pragma unroll
            for (int i = 0; i < 4; ++i) glds16(voff, (const char*)w2 + (size_t)(64 * kb + 16 * i) * (D_ * 2) + ft * 64, l0 + u * 4096 + i * 1024); }
        asm volatile("s_waitcnt vmcnt(0)" ::: "memory");
#pragma unroll
        for (int u = 0; u < 3; ++u) { const int un = 3 * gw + u, kb = un / 20, ft = un - kb * 20;
#pragma unroll
            for (int ks = 0; ks < 4; ++ks) { const lds_ptr a = my + u * 4096 + (16 * ks + 4 * h) * 64 + ro;
                ST16(ws + WS_W2 + (size_t)(ft * 108 + 4 * kb + ks) * 1024 + lane * 16, __builtin_bit_cast(u32x4, cat8(lds_tr(a), lds_tr(a + 512)))); } }
    } else {
#pragma unroll
        for (int u = 0; u < 2; ++u) { const int un = 2 * (gw - 180) + u, gp = un / 20, ft = un - gp * 20;
#pragma unroll
            for (int i = 0; i < 5; ++i) glds16(voff, (const char*)wo + (size_t)(80 * gp + 16 * i) * (D_ * 2) + ft * 64, l0 + u * 5120 + i * 1024); }
        asm volatile("s_waitcnt vmcnt(0)" ::: "memory");
#pragma unroll
        for (int u = 0; u < 2; ++u) { const int un = 2 * (gw - 180) + u, gp = un / 20, ft = un - gp * 20;
#pragma unroll
            for (int ks = 0; ks < 5; ++ks) {
                const lds_ptr a = my + u * 5120 + (ks < 4 ? 20 * ks + 4 * h : 40 * h + 16) * 64 + ro;
                ST16(ws + WS_WO + (size_t)(ft * 40 + 5 * gp + ks) * 1024 + lane * 16, __builtin_bit_cast(u32x4, cat8(lds_tr(a), lds_tr(a + (ks < 4 ? 512 : 1280))))); } }
    }
}

__global__ __launch_bounds__(768, 3) void k1_proj(const unsigned short* x, const unsigned short* wqkv, const unsigned short* w1, const unsigned short* b1,
                                                  const unsigned short* w2, const unsigned short* wo, char* ws) {
    extern __shared__ __attribute__((aligned(16))) char smem[];
    const lds_ptr lds = (lds_ptr)smem;
    const int lane = threadIdx.x & 63, wid = RFL(threadIdx.x >> 6);
    const int b = blockIdx.x;
    TS(0, 0);
    if (b >= K1_GEMM_BLOCKS) { k1_retile(lds, (b - K1_GEMM_BLOCKS) * 12 + wid, wid, lane, w2, wo, ws); TS(0, 7); return; }
    const int xcd = b & 7, t = 29 * (xcd & 3) + (b >> 3);
    const int rb = (xcd >> 2) * 4 + (t & 3), cb = t >> 2, row0 = rb * 256;
    const int g0 = cb < 28 ? cb : 29, g1 = cb < 27 ? cb : cb + 1;
    const char* wb = cb < 27 ? (const char*)w1 + g1 * 128 : (const char*)wqkv + g1 * 128;
    if (wid >= 8) {
        k1_loader(lds, wid - 8, lane, row0, x, (const char*)wqkv + g0 * 128, 3 * D_ * 2, wb, cb < 27 ? F_ * 2 : 3 * D_ * 2, b1 + (cb < 27 ? g1 * 64 : 0), K1_ROT, ws);
    } else {
        const int l = lane & 31, k = 64 * (2 + wid) + (l >> 1) * 4 + (t & 3);
        const char* touch = lane < 32 ? (const char*)x + (size_t)(row0 + l * 8 + (cb & 7)) * (D_ * 2) + (2 + wid) * 128
                          : (l & 1) ? wb + (size_t)k * (cb < 27 ? F_ * 2 : 3 * D_ * 2) : (const char*)wqkv + g0 * 128 + (size_t)k * (3 * D_ * 2);
        k1_compute(lds, wid, lane, row0, cb, g0, g1, x, ws, touch);
    }
    TS(0, 7);
}

#define AT_SYNC WAITV_BAR(0)
#define AT_ROT 0
constexpr int AT_CH = 4 * 3072;
constexpr int AT_STAGE = 2 * AT_CH, AT_NST = 3, AT_LDS = AT_STAGE * AT_NST;

__device__ __forceinline__ f32x16 attn_fallback(const char* kvh, const char* q2, int lane) {
    const int r32 = lane & 31, h = lane >> 5;
    const bf16x8 qa = *(const bf16x8*)(q2 + lane * 16); bf16x4 qb = *(const bf16x4*)(q2 + 1024 + r32 * 8);
    if (h) qb = (bf16x4){0, 0, 0, 0};
    f32x16 o = f32x16{}; float m = -INFINITY;
    const int vrow = r32 < 21 ? r32 : 21;
#pragma unroll 1
    for (int t = 0; t < 64; ++t) {
        const char* kt = kvh + (size_t)t * KVT_B;
        const bf16x8 ka = *(const bf16x8*)(kt + lane * 16); const bf16x4 kb = *(const bf16x4*)(kt + 1024 + lane * 8);
        const bf16x8 v0 = *(const bf16x8*)(kt + 1536 + (h * 24 + vrow) * 16), v1 = *(const bf16x8*)(kt + 1536 + 768 + (h * 24 + vrow) * 16);
        f32x16 s = MFMA16(ka, qa, f32x16{}); s = MFMA8(kb, qb, s);
        float tm = s[0];
#pragma unroll
        for (int i = 1; i < 16; ++i) tm = __builtin_fmaxf(tm, s[i]);
        tm = xhalf_max(tm);
        const float mn = __builtin_fmaxf(m, tm);
        const float al = m == mn ? 1.0f : EX2(m - mn);
        m = mn;
#pragma unroll
        for (int i = 0; i < 16; ++i) { o[i] *= al; s[i] = EX2(s[i] - mn); }
        const u32x4 p0 = {pk2(s[0], s[1]), pk2(s[2], s[3]), pk2(s[4], s[5]), pk2(s[6], s[7])}, p1 = {pk2(s[8], s[9]), pk2(s[10], s[11]), pk2(s[12], s[13]), pk2(s[14], s[15])};
        o = MFMA16(v0, __builtin_bit_cast(bf16x8, p0), o); o = MFMA16(v1, __builtin_bit_cast(bf16x8, p1), o);
    }
    return o;
}

__device__ __forceinline__ void attn_block(lds_ptr lds, int bid, char* ws) {
    const int lane = threadIdx.x & 63, wid = RFL(threadIdx.x >> 6);
    const int r32 = lane & 31, h = lane >> 5;
    const int head = (bid & 7) * 4 + (bid >> 6), qblk = (bid >> 3) & 7;
    const int qg = wid & 3, kh = wid >> 2;
    const char* kvh = ws + WS_KV + (size_t)head * 64 * KVT_B;
    const unsigned l0 = (unsigned)(uintptr_t)lds;
    const char* src = kvh + (size_t)kh * 32 * KVT_B + qg * 3072;
    const unsigned dst = l0 + kh * AT_CH + qg * 3072, lo16 = lane * 16;
    auto issue = [&](int c, int slot) {
#pragma unroll
        for (int i = 0; i < 3; ++i) glds16(lo16, src + (size_t)((c + AT_ROT) & 7) * AT_CH + i * 1024, dst + slot * AT_STAGE + i * 1024);
    };
    const int qt0 = qblk * 8 + qg * 2;
    const char* qp = ws + WS_Q + (size_t)(head * 64 + qt0) * QT_B;
    bf16x8 qa0 = *(const bf16x8*)(qp + lane * 16), qa1 = *(const bf16x8*)(qp + QT_B + lane * 16);
    bf16x4 qb0 = *(const bf16x4*)(qp + 1024 + r32 * 8), qb1 = *(const bf16x4*)(qp + QT_B + 1024 + r32 * 8);
    { const bf16x8 ka = *(const bf16x8*)(kvh + lane * 16); const bf16x4 kb = *(const bf16x4*)(kvh + 1024 + lane * 8);
      issue(0, 0); issue(1, 1);
      if (h) { qb0 = (bf16x4){0, 0, 0, 0}; qb1 = (bf16x4){0, 0, 0, 0}; }
      f32x16 s0 = MFMA16(ka, qa0, f32x16{}); s0 = MFMA8(kb, qb0, s0);
      f32x16 s1 = MFMA16(ka, qa1, f32x16{}); s1 = MFMA8(kb, qb1, s1);
      float m0 = s0[0], m1 = s1[0];
#pragma unroll
      for (int i = 1; i < 16; ++i) { m0 = __builtin_fmaxf(m0, s0[i]); m1 = __builtin_fmaxf(m1, s1[i]); }
      m0 = xhalf_max(m0); m1 = xhalf_max(m1);
      if (h) { qb0[0] = (short)(pk2(-m0, 0.f) & 0xffffu); qb1[0] = (short)(pk2(-m1, 0.f) & 0xffffu); } }
    const int vrow = r32 < 21 ? r32 : 21;
    const lds_ptr kap = lds + kh * AT_CH + lane * 16, kbp = lds + kh * AT_CH + 1024 + lane * 8, vp = lds + kh * AT_CH + 1536 + (h * 24 + vrow) * 16;
    f32x16 o0 = f32x16{}, o1 = f32x16{}, s0, s1;
    u32x4 p0a, p0b, p1a = {0, 0, 0, 0}, p1b = {0, 0, 0, 0};
    bf16x8 ka, va = {0, 0, 0, 0, 0, 0, 0, 0}, vb = {0, 0, 0, 0, 0, 0, 0, 0}; bf16x4 kb;
    WAITV_BAR(3);
    TS(1, 1);
    ka = lds_r128(kap); kb = lds_r64(kbp);
    s0 = MFMA16(ka, qa0, f32x16{}); s0 = MFMA8(kb, qb0, s0);
#define G4(MF, X, i, P, j) do { MF; X[i] = EX2(X[i]); X[i + 1] = EX2(X[i + 1]); X[i + 2] = EX2(X[i + 2]); X[i + 3] = EX2(X[i + 3]); \
        P[j] = pk2(X[i], X[i + 1]); P[j + 1] = pk2(X[i + 2], X[i + 3]); PIN(P); SBAR(); } while (0)
    int slot = 0;
#pragma unroll 1
    for (int c = 0; c < 8; ++c) {
        const int sb = slot * AT_STAGE; const int nslot = slot == 2 ? 0 : slot + 1;
#pragma unroll
        for (int tt = 0; tt < 4; ++tt) {
#define LDK(o) do { ka = lds_r128(kap + (o)); kb = lds_r64(kbp + (o)); } while (0)
#define LDV(o) do { va = lds_r128(vp + (o)); vb = lds_r128(vp + (o) + 768); } while (0)
#define NEXTK() do { if (tt == 3) { if (c < 7) { AT_SYNC; if (c + 2 < 8) issue(c + 2, nslot == 2 ? 0 : nslot + 1); LDK(nslot * AT_STAGE); } } else LDK(sb + (tt + 1) * 3072); } while (0)
            G4(s1 = MFMA16(ka, qa1, f32x16{}), s0, 0, p0a, 0);
            G4(s1 = MFMA8(kb, qb1, s1), s0, 4, p0a, 2);
            NEXTK();
            G4(o1 = MFMA16(va, __builtin_bit_cast(bf16x8, p1a), o1), s0, 8, p0b, 0);
            G4(o1 = MFMA16(vb, __builtin_bit_cast(bf16x8, p1b), o1), s0, 12, p0b, 2);
            LDV(sb + tt * 3072);
            G4(s0 = MFMA16(ka, qa0, f32x16{}), s1, 0, p1a, 0);
            G4(s0 = MFMA8(kb, qb0, s0), s1, 4, p1a, 2);
            G4(o0 = MFMA16(va, __builtin_bit_cast(bf16x8, p0a), o0), s1, 8, p1b, 0);
            G4(o0 = MFMA16(vb, __builtin_bit_cast(bf16x8, p0b), o0), s1, 12, p1b, 2);
        }
        slot = nslot;
    }
    o1 = MFMA16(va, __builtin_bit_cast(bf16x8, p1a), o1); o1 = MFMA16(vb, __builtin_bit_cast(bf16x8, p1b), o1);
#undef G4
    TS(1, 2);
    BAR_LGKM();
    LAS f32x4* ex = (LAS f32x4*)lds + (wid * 3) * 64 + lane;
    { const f32x16 give = kh ? o0 : o1;
#pragma unroll
      for (int g = 0; g < 3; ++g) ex[g * 64] = (f32x4){give[4 * g], give[4 * g + 1], give[4 * g + 2], give[4 * g + 3]}; }
    BAR_LGKM();
    f32x16 o = kh ? o1 : o0;
    { const LAS f32x4* ey = (const LAS f32x4*)lds + ((wid ^ 4) * 3) * 64 + lane;
#pragma unroll
      for (int g = 0; g < 3; ++g) { const f32x4 a = ey[g * 64];
#pragma unroll
        for (int e = 0; e < 4; ++e) o[4 * g + e] += a[e]; } }
    float l = xhalf_sum(h ? o[8] : 0.f);
    float chk = 0.f;
#pragma unroll
    for (int i = 0; i < 12; ++i) chk = __builtin_fmaf(o[i], 0.f, chk);
    const bool bad = !(l > 8.6736174e-19f && l < 1.1529215e18f && chk == 0.f);
    if (__builtin_expect(__any(bad), 0)) {
        o = attn_fallback(kvh, qp + kh * QT_B, lane);
        l = xhalf_sum(h ? o[8] : 0.f);
    }
    const float rl = RCP(l);
    char* ob = ws + WS_O + (size_t)((qt0 + kh) * 40 + 5 * (head >> 2)) * 1024;
    *(u32x4*)(ob + (head & 3) * 1024 + lane * 16) = (u32x4){pk2(o[0] * rl, o[1] * rl), pk2(o[2] * rl, o[3] * rl), pk2(o[4] * rl, o[5] * rl), pk2(o[6] * rl, o[7] * rl)};
    if (!h) *(u32x2*)(ob + 4096 + ((head >> 1) & 1) * 512 + r32 * 16 + (head & 1) * 8) = (u32x2){pk2(o[8] * rl, o[9] * rl), pk2(o[10] * rl, o[11] * rl)};
}

#define M2_ROT 0
#define GLD(dst, voff, sbase, imm) asm volatile("global_load_dwordx4 %0, %1, %2 offset:" #imm : "=v"(dst) : "v"(voff), "s"(sbase) : "memory")
template <int NT>
__device__ __forceinline__ void mlp2_wave(lds_ptr red, int kq, int lane, const char* xs, const char* wsrc, const unsigned short* b2f, unsigned short* gout, int rot) {
    const int r32 = lane & 31, h = lane >> 5;
    unsigned vo[NT];
#pragma unroll
    for (int i = 0; i < NT; ++i) vo[i] = lane * 16 + i * 108 * 1024;
    f32x16 acc[NT];
#pragma unroll
    for (int i = 0; i < NT; ++i) acc[i] = f32x16{};
    u32x4 xf[3], wf[3][NT];
#define LD_STEP(r, imm) do { GLD(xf[r], vo[0], xs, imm); _Pragma("unroll") for (int i = 0; i < NT; ++i) GLD(wf[r][i], vo[i], wsrc, imm); } while (0)
#define USE_STEP(r) do { PIN(xf[r]); _Pragma("unroll") for (int i = 0; i < NT; ++i) { PIN(wf[r][i]); \
        acc[i] = MFMA16(__builtin_bit_cast(bf16x8, wf[r][i]), __builtin_bit_cast(bf16x8, xf[r]), acc[i]); } SBAR(); } while (0)
#define WAITN(n) asm volatile("s_waitcnt vmcnt(%0)" :: "i"(n) : "memory")
    xs += rot * 3072; wsrc += rot * 3072;
    LD_STEP(0, 0); LD_STEP(1, 1024); LD_STEP(2, 2048);
#pragma unroll 1
    for (int it = 0; it < 8; ++it) {
        const int adv = ++rot == 9 ? -8 * 3072 : 3072;
        xs += adv; wsrc += adv;
        WAITN(2 * (NT + 1)); USE_STEP(0); LD_STEP(0, 0);
        WAITN(2 * (NT + 1)); USE_STEP(1); LD_STEP(1, 1024);
        WAITN(2 * (NT + 1)); USE_STEP(2); LD_STEP(2, 2048);
    }
    WAITN(2 * (NT + 1)); USE_STEP(0); WAITN(NT + 1); USE_STEP(1); WAITN(0); USE_STEP(2);
    LAS f32x4* rb = (LAS f32x4*)red;
#pragma unroll
    for (int d = 1; d < 4; ++d) { const int dq = (kq + d) & 3;
#pragma unroll
        for (int i = 0; i < NT; ++i) {
            const f32x4 v = dq == 0 ? (f32x4){acc[i][0], acc[i][1], acc[i][2], acc[i][3]} : dq == 1 ? (f32x4){acc[i][4], acc[i][5], acc[i][6], acc[i][7]}
                          : dq == 2 ? (f32x4){acc[i][8], acc[i][9], acc[i][10], acc[i][11]} : (f32x4){acc[i][12], acc[i][13], acc[i][14], acc[i][15]};
            rb[((dq * 3 + (d - 1)) * NT + i) * 64 + lane] = v; } }
    BAR_LGKM();
#pragma unroll
    for (int i = 0; i < NT; ++i) {
        f32x4 v = kq == 0 ? (f32x4){acc[i][0], acc[i][1], acc[i][2], acc[i][3]} : kq == 1 ? (f32x4){acc[i][4], acc[i][5], acc[i][6], acc[i][7]}
                : kq == 2 ? (f32x4){acc[i][8], acc[i][9], acc[i][10], acc[i][11]} : (f32x4){acc[i][12], acc[i][13], acc[i][14], acc[i][15]};
#pragma unroll
        for (int d = 0; d < 3; ++d) v += rb[((kq * 3 + d) * NT + i) * 64 + lane];
        const int f = 32 * i + 8 * kq + 4 * h;
        const u32x2 bb = *(const u32x2*)(b2f + f);
        const float u0 = v[0] + bf_lo(bb[0]), u1 = v[1] + bf_hi(bb[0]), u2 = v[2] + bf_lo(bb[1]), u3 = v[3] + bf_hi(bb[1]);
#define SILU(u) ((u) * RCP(1.0f + EX2((u) * -LOG2E)))
        *(u32x2*)(gout + r32 * D_ + f) = (u32x2){pk2(SILU(u0), SILU(u1)), pk2(SILU(u2), SILU(u3))};
    }
}
__device__ __forceinline__ void mlp2_block(lds_ptr lds, int bid, const unsigned short* b2, char* ws) {
    const int lane = threadIdx.x & 63, wid = RFL(threadIdx.x >> 6), kq = wid & 3;
    const int tt = (bid & 7) * 8 + ((bid >> 3) & 7), cg = bid >> 6;
    const char* xs = ws + WS_H + (size_t)(tt * 108 + 27 * kq) * 1024;
    const int ft = 5 * cg + (wid < 4 ? 0 : 3);
    const char* wsrc = ws + WS_W2 + (size_t)(ft * 108 + 27 * kq) * 1024;
    unsigned short* gout = (unsigned short*)(ws + WS_G) + (size_t)tt * 32 * D_ + ft * 32;
    if (wid < 4) mlp2_wave<3>(lds, kq, lane, xs, wsrc, b2 + ft * 32, gout, M2_ROT);
    else mlp2_wave<2>(lds + 36864, kq, lane, xs, wsrc, b2 + ft * 32, gout, M2_ROT);
}

__global__ __launch_bounds__(512, 4) void k2_attn_mlp2(const unsigned short* b2, char* ws, int mode) {
    extern __shared__ __attribute__((aligned(16))) char smem[];
    const int b = blockIdx.x + mode;
    TS(1 + (mode != 0), 0);
    if (b < 256) attn_block((lds_ptr)smem, b, ws); else mlp2_block((lds_ptr)smem, b - 256, b2, ws);
    TS(1 + (mode != 0), 7);
}

#define K3_ROT 0
constexpr int K3_ROW = 336, K3_PART = 64 * K3_ROW, K3_LDS = 5 * K3_PART;
__global__ __launch_bounds__(320, 1) void k3_out(const unsigned short* x, char* ws, unsigned short* out) {
    TS(3, 0);
    extern __shared__ __attribute__((aligned(16))) char smem[];
    const lds_ptr lds = (lds_ptr)smem;
    const int lane = threadIdx.x & 63, wid = RFL(threadIdx.x >> 6), bid = blockIdx.x;
    const int xcd = bid & 7, i5 = bid >> 3;
    const int tb = RFL((xcd >> 1) * 8 + (i5 & 7)), fg = RFL((xcd & 1) * 4 + (i5 >> 3));
    const int c16 = lane & 15, g = lane >> 4;
    const unsigned vo = (g >> 1) * 1024 + (g & 1) * 512 + c16 * 16;
    const char* xs = uni(ws + WS_O + (size_t)(tb * 2 * 40 + 8 * wid) * 1024);
    const char* wsrc = uni(ws + WS_WO + (size_t)(8 * wid) * 1024);
    u32x4 xf[4][4], wf[4][5];
#pragma unroll
    for (int j0 = 0; j0 < 4; ++j0) {
        const int j = j0, jr = (j0 + K3_ROT) & 3;
#pragma unroll
        for (int i = 0; i < 5; ++i) { const int f16 = fg * 5 + i;
            asm volatile("global_load_dwordx4 %0, %1, %2" : "=v"(wf[j][i]) : "v"(vo), "s"(uni(wsrc + (size_t)((f16 >> 1) * 40 + 2 * jr) * 1024 + (f16 & 1) * 256)) : "memory"); }
#pragma unroll
        for (int t = 0; t < 4; ++t)
            asm volatile("global_load_dwordx4 %0, %1, %2" : "=v"(xf[j][t]) : "v"(vo), "s"(uni(xs + (size_t)((t >> 1) * 40 + 2 * jr) * 1024 + (t & 1) * 256)) : "memory");
    }
    const int t = threadIdx.x, tok = (t * 13108) >> 16, c = t - tok * 5;
    const size_t eo = (size_t)(tb * 64 + tok) * D_ + fg * 80 + c * 16;
    u32x4 xv[2], gv[2];
    { const unsigned short* gp = (const unsigned short*)(ws + WS_G);
      asm volatile("global_load_dwordx4 %0, %1, off" : "=v"(xv[0]) : "v"(x + eo) : "memory");
      asm volatile("global_load_dwordx4 %0, %1, off offset:16" : "=v"(xv[1]) : "v"(x + eo) : "memory");
      asm volatile("global_load_dwordx4 %0, %1, off" : "=v"(gv[0]) : "v"(gp + eo) : "memory");
      asm volatile("global_load_dwordx4 %0, %1, off offset:16" : "=v"(gv[1]) : "v"(gp + eo) : "memory"); }
    f32x4 acc[4][5];
#pragma unroll
    for (int a = 0; a < 4; ++a)
#pragma unroll
        for (int i = 0; i < 5; ++i) acc[a][i] = (f32x4){0.f, 0.f, 0.f, 0.f};
#pragma unroll
    for (int j = 0; j < 4; ++j) {
        asm volatile("s_waitcnt vmcnt(%0)" :: "i"(4 + 9 * (3 - j)) : "memory");
#pragma unroll
        for (int i = 0; i < 5; ++i) PIN(wf[j][i]);
#pragma unroll
        for (int a = 0; a < 4; ++a) { PIN(xf[j][a]);
#pragma unroll
            for (int i = 0; i < 5; ++i) acc[a][i] = __builtin_amdgcn_mfma_f32_16x16x32_bf16(__builtin_bit_cast(bf16x8, wf[j][i]), __builtin_bit_cast(bf16x8, xf[j][a]), acc[a][i], 0, 0, 0); }
        SBAR();
    }
#pragma unroll
    for (int a = 0; a < 4; ++a)
#pragma unroll
        for (int i = 0; i < 5; ++i) *(LAS f32x4*)(lds + wid * K3_PART + (16 * a + c16) * K3_ROW + (16 * i + 4 * g) * 4) = acc[a][i];
    TS(3, 1);
    WAITV_BAR(0);
    TS(3, 2);
    PIN(xv[0]); PIN(xv[1]); PIN(gv[0]); PIN(gv[1]);
#pragma unroll
    for (int j = 0; j < 2; ++j) {
        u32x4 o;
#pragma unroll
        for (int e = 0; e < 2; ++e) {
            f32x4 s = *(const LAS f32x4*)(lds + tok * K3_ROW + c * 64 + j * 32 + e * 16);
#pragma unroll
            for (int p = 1; p < 5; ++p) s += *(const LAS f32x4*)(lds + p * K3_PART + tok * K3_ROW + c * 64 + j * 32 + e * 16);
            o[2 * e] = pk2(__builtin_fmaf(s[0], bf_lo(gv[j][2 * e]), bf_lo(xv[j][2 * e])), __builtin_fmaf(s[1], bf_hi(gv[j][2 * e]), bf_hi(xv[j][2 * e])));
            o[2 * e + 1] = pk2(__builtin_fmaf(s[2], bf_lo(gv[j][2 * e + 1]), bf_lo(xv[j][2 * e + 1])), __builtin_fmaf(s[3], bf_hi(gv[j][2 * e + 1]), bf_hi(xv[j][2 * e + 1])));
        }
        *(u32x4*)(out + eo + j * 8) = o;
    }
    TS(3, 7);
}

extern "C" void kernel_launch(void* const* d_in, const int* in_sizes, int n_in, void* d_out, int out_size, void* d_ws, size_t ws_size, hipStream_t stream) {
    const unsigned short* x = (const unsigned short*)d_in[0]; const unsigned short* wqkv = (const unsigned short*)d_in[1];
    const unsigned short* wo = (const unsigned short*)d_in[2]; const unsigned short* w1 = (const unsigned short*)d_in[4];
    const unsigned short* b1 = (const unsigned short*)d_in[5]; const unsigned short* w2 = (const unsigned short*)d_in[6];
    const unsigned short* b2 = (const unsigned short*)d_in[7];
    char* ws = (char*)d_ws;
    (void)hipFuncSetAttribute((const void*)k1_proj, hipFuncAttributeMaxDynamicSharedMemorySize, K1_LDS);
    (void)hipFuncSetAttribute((const void*)k2_attn_mlp2, hipFuncAttributeMaxDynamicSharedMemorySize, AT_LDS);
    (void)hipFuncSetAttribute((const void*)k3_out, hipFuncAttributeMaxDynamicSharedMemorySize, K3_LDS);
    k1_proj<<<256, 768, K1_LDS, stream>>>(x, wqkv, w1, b1, w2, wo, ws);
    k2_attn_mlp2<<<512, 512, AT_LDS, stream>>>(b2, ws, 0);
    k3_out<<<256, 320, K3_LDS, stream>>>(x, ws, (unsigned short*)d_out);
}
```

```cpp
#include <hip/hip_runtime.h>
#include <hip/hip_bf16.h>
#include <stdint.h>

typedef short bf16x8 __attribute__((ext_vector_type(8)));
typedef short bf16x4 __attribute__((ext_vector_type(4)));
typedef float f32x16 __attribute__((ext_vector_type(16)));
typedef float f32x4 __attribute__((ext_vector_type(4)));
typedef float f32x2 __attribute__((ext_vector_type(2)));
typedef unsigned u32x4 __attribute__((ext_vector_type(4)));
typedef unsigned u32x2 __attribute__((ext_vector_type(2)));
typedef __bf16 bf16x2_t __attribute__((ext_vector_type(2)));
typedef short v4i16_t __attribute__((ext_vector_type(4)));
#define LAS __attribute__((address_space(3)))
typedef LAS char* lds_ptr;

constexpr int S_ = 2048, D_ = 640, F_ = 1728, NH = 32;
constexpr float LN_EPS = 1e-5f;
constexpr float LOG2E = 1.4426950408889634f;
constexpr float QSCALE = 0.22360679774997896f * LOG2E;

constexpr size_t QT_B = 1280, KVT_B = 3072;
constexpr size_t WS_Q = 0;
constexpr size_t WS_KV = WS_Q + (size_t)NH * 64 * QT_B;
constexpr size_t WS_H = WS_KV + (size_t)NH * 64 * KVT_B;
constexpr size_t WS_O = WS_H + (size_t)64 * 108 * 1024;
constexpr size_t WS_G = WS_O + (size_t)64 * 40 * 1024;
constexpr size_t WS_W2 = WS_G + (size_t)S_ * D_ * 2;
constexpr size_t WS_WO = WS_W2 + (size_t)20 * 108 * 1024;
constexpr size_t WS_ST = WS_WO + (size_t)20 * 40 * 1024;
constexpr size_t WS_EP = WS_ST + (size_t)S_ * 16 + 4096;
constexpr size_t WS_END = WS_EP + 4096;

#define TS(k, slot) do {} while (0)
#define MFMA16(a, b, c) __builtin_amdgcn_mfma_f32_32x32x16_bf16(a, b, c, 0, 0, 0)
#define MFMA8(a, b, c) __builtin_amdgcn_mfma_f32_32x32x8bf16_1k(a, b, c, 0, 0, 0)
#define SBAR() __builtin_amdgcn_sched_barrier(0)
#define PIN(x) asm volatile("" : "+v"(x))
#define RFL(x) __builtin_amdgcn_readfirstlane(x)

__device__ __forceinline__ const char* uni(const void* p) {
    const unsigned long long a = (unsigned long long)p; return (const char*)(((unsigned long long)(unsigned)RFL((unsigned)(a >> 32)) << 32) | (unsigned)RFL((unsigned)a)); }
__device__ __forceinline__ unsigned pk2(float a, float b) { f32x2 v = {a, b}; return __builtin_bit_cast(unsigned, __builtin_convertvector(v, bf16x2_t)); }
__device__ __forceinline__ unsigned pk2v(f32x2 v) { return __builtin_bit_cast(unsigned, __builtin_convertvector(v, bf16x2_t)); }
__device__ __forceinline__ float bf_lo(unsigned u) { return __uint_as_float(u << 16); }
__device__ __forceinline__ float bf_hi(unsigned u) { return __uint_as_float(u & 0xffff0000u); }
__device__ __forceinline__ float xhalf_sum(float v) {
    auto r = __builtin_amdgcn_permlane32_swap(__float_as_uint(v), __float_as_uint(v), false, false);
    return __uint_as_float(r[0]) + __uint_as_float(r[1]); }
__device__ __forceinline__ float xhalf_max(float v) {
    auto r = __builtin_amdgcn_permlane32_swap(__float_as_uint(v), __float_as_uint(v), false, false);
    return __builtin_fmaxf(__uint_as_float(r[0]), __uint_as_float(r[1])); }
__device__ __forceinline__ void glds16(unsigned voff, const void* sbase, unsigned lds_base) {
    unsigned sv; asm volatile("s_mov_b32 %0, m0\n\ts_mov_b32 m0, %3\n\ts_nop 0\n\tglobal_load_lds_dwordx4 %1, %2\n\ts_mov_b32 m0, %0"
                              : "=&s"(sv) : "v"(voff), "s"(sbase), "s"(lds_base) : "memory"); }
__device__ __forceinline__ bf16x8 lds_r128(lds_ptr p) { return *(const LAS bf16x8*)p; }
__device__ __forceinline__ bf16x4 lds_r64(lds_ptr p) { return *(const LAS bf16x4*)p; }
__device__ __forceinline__ bf16x4 lds_tr(lds_ptr p) { return __builtin_bit_cast(bf16x4, __builtin_amdgcn_ds_read_tr16_b64_v4i16((LAS v4i16_t*)p)); }
__device__ __forceinline__ bf16x8 cat8(bf16x4 a, bf16x4 b) { return (bf16x8){a[0], a[1], a[2], a[3], b[0], b[1], b[2], b[3]}; }
#define WAITV_BAR(N) asm volatile("s_waitcnt vmcnt(" #N ") lgkmcnt(0)\n\ts_barrier" ::: "memory")
#define BAR_LGKM() asm volatile("s_waitcnt lgkmcnt(0)\n\ts_barrier" ::: "memory")
#define EX2(v) __builtin_amdgcn_exp2f(v)
#define RCP(v) __builtin_amdgcn_rcpf(v)

constexpr int K1_STAGE = 49152, K1_NST = 3, K1_STATS = K1_STAGE * K1_NST, K1_LDS = K1_STATS + 4 * 1024 + 4 * 512 + 256 + 512 + 256 + 256;
constexpr int K1_GEMM_BLOCKS = 232;
#define TD 4
#define TMODE 0
#define ST_TRIES 64
__device__ __forceinline__ unsigned epoch_tag(unsigned e) { return e * 2654435761u + 0x7F4A7C15u; }
#define K1_ROT 0

#define TOUCH 0
#define STATS_MODE 0
#define ST16(p, v) asm volatile("global_store_dwordx4 %0, %1, off sc1\n\ts_nop 1" :: "v"(p), "v"(v) : "memory")
__device__ __forceinline__ void acc_sum(float& s, float v) { asm("v_add_f32 %0, %0, %1" : "+v"(s) : "v"(v)); }
__device__ __forceinline__ void acc_sq(float& s, float v) { asm("v_fmac_f32 %0, %1, %1" : "+v"(s) : "v"(v)); }
__device__ __forceinline__ float dot2(unsigned a, unsigned b, float c) {
    return __builtin_amdgcn_fdot2_f32_bf16(__builtin_bit_cast(bf16x2_t, a), __builtin_bit_cast(bf16x2_t, b), c, false); }

template <bool V0, bool V1>
__device__ __forceinline__ void k1_main(lds_ptr lds, int wf, int wt, int lane, f32x16 (&acc)[2][2]) {
    const int r32 = lane & 31, h = lane >> 5;
    unsigned xo[4][2], wa[4];
    { const int blk = (lane >> 4) & 1, q = (lane & 15) >> 2, p = lane & 3, ch = 4 * wf + 2 * blk + (p >> 1);
      const unsigned l0 = (unsigned)(uintptr_t)lds, a = l0 + 32768 + (8 * h + q) * 128 + 16 * (ch ^ ((q >> 1) << 2)) + 8 * (p & 1);
#pragma unroll
      for (int ks = 0; ks < 4; ++ks) { wa[ks] = a + (ks ^ wt) * 2048;
#pragma unroll
          for (int jp = 0; jp < 2; ++jp) { const int row = 64 * wt + 32 * (jp ^ wf) + r32; xo[ks][jp] = l0 + row * 128 + (((2 * (ks ^ wt) + h) ^ ((row >> 1) & 7)) << 4); } } }
#pragma unroll
    for (int a = 0; a < 2; ++a)
#pragma unroll
        for (int b = 0; b < 2; ++b) acc[a][b] = f32x16{};
    f32x4 c0 = {0.f, 0.f, 0.f, 0.f}, c1 = c0;
    const bf16x4 ones = {0x3F80, 0x3F80, 0x3F80, 0x3F80};
    bf16x8 fx0[2], fx1[2], fw0[2], fw1[2];
#define LP(a) ((lds_ptr)(uintptr_t)(a))
#define K1_LD(B, ks) do { fx0[B] = lds_r128(LP(xo[ks][0])); fw0[B] = cat8(lds_tr(LP(wa[ks])), lds_tr(LP(wa[ks]) + 512)); \
        fx1[B] = lds_r128(LP(xo[ks][1])); fw1[B] = cat8(lds_tr(LP(wa[ks]) + 8192), lds_tr(LP(wa[ks]) + 8192 + 512)); SBAR(); } while (0)
#define SUM4(c, f, o) do { c = __builtin_amdgcn_mfma_f32_4x4x4bf16_1k(ones, __builtin_shufflevector(f, f, o, o + 1, o + 2, o + 3), c, 0, 0, 0); SBAR(); } while (0)
#define K1_MM(B, ks) do { \
        acc[0][0] = V0 ? MFMA16(fx0[B], fw0[B], acc[0][0]) : MFMA16(fw0[B], fx0[B], acc[0][0]); SBAR(); if (ks == 0) SUM4(c0, fw0[B], 0); \
        acc[0][1] = V0 ? MFMA16(fx1[B], fw0[B], acc[0][1]) : MFMA16(fw0[B], fx1[B], acc[0][1]); SBAR(); if (ks == 0) SUM4(c0, fw0[B], 4); if (ks == 1) SUM4(c1, fw1[B], 0); \
        acc[1][0] = V1 ? MFMA16(fx0[B], fw1[B], acc[1][0]) : MFMA16(fw1[B], fx0[B], acc[1][0]); SBAR(); if (ks == 1) SUM4(c1, fw1[B], 4); \
        acc[1][1] = V1 ? MFMA16(fx1[B], fw1[B], acc[1][1]) : MFMA16(fw1[B], fx1[B], acc[1][1]); SBAR(); } while (0)
    BAR_LGKM();
    K1_LD(0, 0);
    int slot = 0;
#pragma unroll 1
    for (int s = 0; s < 9; ++s) {
        const int adv = slot == K1_NST - 1 ? -(K1_NST - 1) * K1_STAGE : K1_STAGE; slot = slot == K1_NST - 1 ? 0 : slot + 1;
        K1_LD(1, 1); xo[0][0] += adv; xo[0][1] += adv; wa[0] += adv; xo[1][0] += adv; xo[1][1] += adv; wa[1] += adv;
        K1_MM(0, 0);
        K1_LD(0, 2); xo[2][0] += adv; xo[2][1] += adv; wa[2] += adv;
        K1_MM(1, 1);
        K1_LD(1, 3); xo[3][0] += adv; xo[3][1] += adv; wa[3] += adv;
        K1_MM(0, 2);
        BAR_LGKM(); K1_LD(0, 0);
        K1_MM(1, 3);
    }
    K1_LD(1, 1); K1_MM(0, 0); K1_LD(0, 2); K1_MM(1, 1); K1_LD(1, 3);
    { const float a = xhalf_sum(c0[0]), b = xhalf_sum(c1[0]);
      ((LAS float*)(lds + K1_STATS))[1024 + wt * 128 + 64 * h + 32 * wf + r32] = h ? b : a; BAR_LGKM(); }
    K1_MM(0, 2); K1_MM(1, 3);
#undef K1_LD
#undef K1_MM
}

__device__ __forceinline__ void k1_store_v(const f32x16 (&acc)[2], const LAS float* s_r, const LAS float* s_cs, int wf, int wt, int lane, int row0, int grp, char* ws) {
    const int r32 = lane & 31, h = lane >> 5;
    const int fl = 32 * wf + r32, f = 64 * (grp - 20) + fl, hd = (f * 3277) >> 16, d = f - hd * 20;
    const float c = -s_cs[fl];
#pragma unroll
    for (int jp = 0; jp < 2; ++jp) {
        const int tl = 64 * wt + 32 * (jp ^ wf);
        char* base = ws + WS_KV + (size_t)(hd * 64 + ((row0 + tl) >> 5)) * KVT_B + 1536 + (h * 24 + d) * 16;
#pragma unroll
        for (int s = 0; s < 2; ++s) {
            u32x4 o;
#pragma unroll
            for (int gg = 0; gg < 2; ++gg) { const int g = 2 * s + gg;
                const f32x4 r4 = *(const LAS f32x4*)(s_r + tl + 8 * g + 4 * h), m4 = *(const LAS f32x4*)(s_r + 256 + tl + 8 * g + 4 * h);
#pragma unroll
                for (int e = 0; e < 2; ++e) { const f32x2 a = {acc[jp][4 * g + 2 * e], acc[jp][4 * g + 2 * e + 1]}, r2 = {r4[2 * e], r4[2 * e + 1]}, m2 = {m4[2 * e], m4[2 * e + 1]};
                    o[2 * gg + e] = pk2v(__builtin_elementwise_fma(a, r2, m2 * c)); } }
            ST16(base + s * 768, o);
            if (d >= 18) { const unsigned k = d == 19 ? 0x3F803F80u : 0u; const u32x4 kk = {k, k, k, k};
                ST16(base + s * 768 + (d == 19 ? 16 : 48), kk); }
        }
    }
}

__device__ __forceinline__ void k1_compute(lds_ptr lds, int wid, int lane, int row0, int cb, int g0, int g1, const unsigned short* x, char* ws) {
    const int wf = wid >> 2, wt = wid & 3, r32 = lane & 31, h = lane >> 5;
    f32x16 acc[2][2];
    if (cb < 20) k1_main<false, false>(lds, wf, wt, lane, acc);
    else if (cb < 27) k1_main<true, false>(lds, wf, wt, lane, acc);
    else k1_main<true, true>(lds, wf, wt, lane, acc);
    TS(0, 2);
    const LAS float* s_r = (const LAS float*)(lds + K1_STATS);
    const LAS float* s_b1 = s_r + 1536; const LAS float* s_cs = s_r + 1600;
    BAR_LGKM();
    TS(0, 3);
    if (cb < 20) {
        float rr[2], rm[2];
        const bool isq = cb < 10;
#pragma unroll
        for (int jp = 0; jp < 2; ++jp) { const int t = 64 * wt + 32 * (jp ^ wf) + r32; const float sc = isq ? QSCALE : 1.0f; rr[jp] = s_r[t] * sc; rm[jp] = -s_r[256 + t] * sc; }
        char* base = isq ? ws + WS_Q : ws + WS_KV; const int tb = isq ? (int)QT_B : (int)KVT_B;
#pragma unroll
        for (int g = 0; g < 4; ++g) {
            const int fl = 32 * wf + 8 * g + 4 * h, f0 = 64 * (isq ? cb : cb - 10) + fl;
            const f32x4 c4 = *(const LAS f32x4*)(s_cs + fl);
            const int hd = (f0 * 3277) >> 16, d0 = f0 - hd * 20;
            const int off = hd * 64 * tb + (d0 < 16 ? (d0 >> 3) * 512 + r32 * 16 + (d0 & 4) * 2 : 1024 + r32 * 8);
#pragma unroll
            for (int jp = 0; jp < 2; ++jp) {
                const int tile = (row0 >> 5) + 2 * wt + (jp ^ wf);
                u32x2 o;
#pragma unroll
                for (int e = 0; e < 2; ++e) { const f32x2 a = {acc[0][jp][4 * g + 2 * e], acc[0][jp][4 * g + 2 * e + 1]}, c2 = {c4[2 * e], c4[2 * e + 1]};
                    o[e] = pk2v(__builtin_elementwise_fma(a, (f32x2){rr[jp], rr[jp]}, c2 * rm[jp])); }
                char* p = base + off + tile * tb;
                *(u32x2*)p = o;
                if (!isq && d0 == 16) *(u32x2*)(p + 256) = (u32x2){0x00003F80u, 0u};
            }
        }
    } else k1_store_v(acc[0], s_r, s_cs, wf, wt, lane, row0, g0, ws);
    TS(0, 1);
    if (cb < 27) {
        constexpr float C1 = -2.0f * LOG2E * 0.7978845608028654f, C2 = C1 * 0.044715f;
        float a2[2], m2[2];
#pragma unroll
        for (int jp = 0; jp < 2; ++jp) { const int t = 64 * wt + 32 * (jp ^ wf) + r32; a2[jp] = s_r[512 + t]; m2[jp] = -s_r[768 + t]; }
#pragma unroll
        for (int s = 0; s < 2; ++s) {
            u32x4 o[2];
#pragma unroll
            for (int gg = 0; gg < 2; ++gg) {
                const int g = 2 * s + gg, fl = 32 * wf + 8 * g + 4 * h;
                const f32x4 c4 = *(const LAS f32x4*)(s_cs + 64 + fl), b4 = *(const LAS f32x4*)(s_b1 + fl);
#pragma unroll
                for (int jp = 0; jp < 2; ++jp) {
#pragma unroll
                    for (int e = 0; e < 2; ++e) {
                        const f32x2 a = {acc[1][jp][4 * g + 2 * e], acc[1][jp][4 * g + 2 * e + 1]}, c2 = {c4[2 * e], c4[2 * e + 1]}, bb = {b4[2 * e], b4[2 * e + 1]};
                        const f32x2 u = __builtin_elementwise_fma(a, (f32x2){a2[jp], a2[jp]}, __builtin_elementwise_fma(c2, (f32x2){m2[jp], m2[jp]}, bb));
                        const f32x2 w = u * __builtin_elementwise_fma(u * u, (f32x2){C2, C2}, (f32x2){C1, C1});
                        const f32x2 d = (f32x2){EX2(w[0]), EX2(w[1])} + 1.0f;
                        o[jp][2 * gg + e] = pk2v(u * (f32x2){RCP(d[0]), RCP(d[1])});
                    }
                }
            }
#pragma unroll
            for (int jp = 0; jp < 2; ++jp) {
                const int tile = (row0 >> 5) + 2 * wt + (jp ^ wf);
                ST16(ws + WS_H + (size_t)(tile * 108 + 4 * g1 + 2 * wf + s) * 1024 + lane * 16, o[jp]);
            }
        }
    } else if (cb == 27) k1_store_v(acc[1], s_r, s_cs + 64, wf, wt, lane, row0, g1, ws);
}

#define DPP_ADD(v, ctrl) v += __uint_as_float(__builtin_amdgcn_mov_dpp(__float_as_uint(v), ctrl, 0xf, 0xf, true))
__device__ __forceinline__ float wave_sum(float v) {
    DPP_ADD(v, 0xB1); DPP_ADD(v, 0x4E); DPP_ADD(v, 0x141); DPP_ADD(v, 0x140);
    auto r = __builtin_amdgcn_permlane16_swap(__float_as_uint(v), __float_as_uint(v), false, false);
    return xhalf_sum(__uint_as_float(r[0]) + __uint_as_float(r[1])); }

__device__ __forceinline__ f32x2 row_stats(u32x4 ra, unsigned rb) {
    float v[10];
#pragma unroll
    for (int j = 0; j < 4; ++j) { v[2 * j] = bf_lo(ra[j]); v[2 * j + 1] = bf_hi(ra[j]); }
    v[8] = bf_lo(rb); v[9] = bf_hi(rb);
    float a = 0.f;
#pragma unroll
    for (int j = 0; j < 10; ++j) a += v[j];
    const float m = wave_sum(a) * (1.0f / D_);
    a = 0.f;
#pragma unroll
    for (int j = 0; j < 10; ++j) a = __builtin_fmaf(v[j] - m, v[j] - m, a);
    return (f32x2){m, wave_sum(a) * (1.0f / D_)};
}

__device__ __forceinline__ void k1_loader(lds_ptr lds, int lw, int lane, int row0, const unsigned short* x, const char* w0, int ld0, const char* w1, int ld1,
                                          const unsigned short* b1g, int gw, char* ws, const char* touch, int tstep, const char* tx, const char* tw) {
    const unsigned l0 = (unsigned)(uintptr_t)lds;
    unsigned xv[2];
#pragma unroll
    for (int pp = 0; pp < 2; ++pp) { const int row = 8 * pp + (lane >> 3); xv[pp] = row * (D_ * 2) + (((lane & 7) ^ ((row >> 1) & 7)) << 4); }
    const int wc = ((lane & 7) ^ (((lane >> 4) & 1) << 2)) << 4;
    const char* wsrc = lw < 2 ? w0 : w1; const int ldw = lw < 2 ? ld0 : ld1;
    const unsigned wv = (lane >> 3) * ldw + wc;
    wsrc += (size_t)(lw & 1) * 32 * ldw;
    const char* xsrc = (const char*)x + (size_t)(row0 + 64 * lw) * (D_ * 2);
    auto issue = [&](int s, int slot) {
        const unsigned base = l0 + slot * K1_STAGE;
#pragma unroll
        for (int i = 0; i < 4; ++i) {
            glds16(wv, wsrc + (size_t)(s * 64 + 8 * i) * ldw, base + 32768 + (4 * lw + i) * 1024);
            glds16(xv[0], xsrc + i * 16 * (D_ * 2) + s * 128, base + (8 * lw + 2 * i) * 1024);
            glds16(xv[1], xsrc + i * 16 * (D_ * 2) + s * 128, base + (8 * lw + 2 * i + 1) * 1024);
        }
    };
    __builtin_amdgcn_s_setprio(3);
    const unsigned tdump = l0 + K1_LDS - 256;
#define K1_TCH(p) do { unsigned sv_; asm volatile("s_mov_b32 %0, m0\n\ts_mov_b32 m0, %2\n\ts_nop 0\n\tglobal_load_lds_dword %1, off\n\ts_mov_b32 m0, %0" \
        : "=&s"(sv_) : "v"(p), "s"(tdump) : "memory"); } while (0)
#define K1_TOUCH() do { K1_TCH(touch); touch += tstep; } while (0)
#define NT_(c) (c)
    TS(2, 0);
    issue(0, 0);
    unsigned bv, ep; asm volatile("global_load_ushort %0, %1, %2" : "=v"(bv) : "v"(lane * 2), "s"(b1g) : "memory");
    asm volatile("global_load_dword %0, %1, %2" : "=v"(ep) : "v"(0), "s"(ws + WS_EP) : "memory");
    issue(1, 1);
#pragma unroll
    for (int i = 2; i < TD; ++i) K1_TOUCH();
    u32x4 st;
    LAS float* s_r = (LAS float*)(lds + K1_STATS);
    const int tok = row0 + 64 * lw + lane; const char* sp = ws + WS_ST + (size_t)tok * 16;
#pragma unroll
    for (int s = 0; s < 10; ++s) {
#define SX 0
#define SW 2
#define NTCH(s) ((s) < 0 ? TD - 2 : ((s) < 10 - TD) + ((s) == SX) + ((s) == SW))
        const int newer = s == 9 ? 1 : 12 + NT_(NTCH(s - 1) + (s >= 1 ? NTCH(s - 2) : 0));
        asm volatile("s_waitcnt vmcnt(%0) lgkmcnt(0)\n\ts_barrier" :: "i"(newer) : "memory");
        if (s + 2 < 10) issue(s + 2, (s + 2) % K1_NST);
        if (s < 10 - TD) K1_TOUCH();
        if (s == SX) K1_TCH(tx);
        if (s == SW) K1_TCH(tw);
        if (s == 8) asm volatile("global_load_dwordx4 %0, %1, off sc1" : "=v"(st) : "v"(sp) : "memory");
    }
    asm volatile("s_waitcnt vmcnt(0)" : "+v"(st), "+v"(ep), "+v"(bv) :: "memory");
    unsigned tag = epoch_tag(ep);
    bool ok = st[1] == tag && st[3] == tag;
    auto finish = [&](float m, float var) {
        const float r = __builtin_amdgcn_rsqf(var + LN_EPS);
        const float a2 = r * __builtin_amdgcn_rsqf(__builtin_fmaf(var * r, r, LN_EPS));
        const int t = 64 * lw + lane;
        s_r[t] = r; s_r[256 + t] = r * m; s_r[512 + t] = a2; s_r[768 + t] = a2 * m; };
    finish(__uint_as_float(st[0]), __uint_as_float(st[2]));
    if (lw == 0) s_r[1536 + lane] = __uint_as_float(bv << 16);
    BAR_LGKM();
    if (lw < 2) { const LAS float* pc = s_r + 1024 + 64 * lw + lane; s_r[1600 + 64 * lw + lane] = (pc[0] + pc[128]) + (pc[256] + pc[384]); }
    if (__builtin_expect(!__all(ok), 0)) {
#pragma unroll 1
    for (int tr = 0; tr < ST_TRIES && !__all(ok); ++tr) {
        asm volatile("global_load_dwordx4 %0, %1, off sc1\n\ts_waitcnt vmcnt(0)" : "=v"(st) : "v"(sp) : "memory");
        ok = st[1] == tag && st[3] == tag;
    }
    float m = __uint_as_float(st[0]), var = __uint_as_float(st[2]);
    if (__builtin_expect(!__all(ok), 0)) {
#pragma unroll 1
        for (int i = 0; i < 64; ++i) { const char* rp = (const char*)x + (size_t)(row0 + 64 * lw + i) * (D_ * 2);
            const f32x2 mv = row_stats(*(const u32x4*)(rp + lane * 16), *(const unsigned*)(rp + 1024 + lane * 4));
            if (lane == i) { m = mv[0]; var = mv[1]; } }
    }
    finish(m, var);
    }
    BAR_LGKM();
    TS(2, 7);
}

__device__ __forceinline__ void k1_retile(lds_ptr lds, int gw, int wid, int lane, const unsigned short* w2, const unsigned short* wo, char* ws) {
    if (gw >= 260) return;
    const lds_ptr my = lds + wid * 12288; const unsigned l0 = (unsigned)(uintptr_t)my;
    const int h = lane >> 5, blk = (lane >> 4) & 1, q = (lane & 15) >> 2, p = lane & 3;
    const unsigned voff = (lane >> 2) * (D_ * 2) + (lane & 3) * 16;
    const unsigned ro = q * 64 + blk * 32 + p * 8;
    if (gw < 180) {
#pragma unroll
        for (int u = 0; u < 3; ++u) { const int un = 3 * gw + u, kb = un / 20, ft = un - kb * 20;
#pragma unroll
            for (int i = 0; i < 4; ++i) glds16(voff, (const char*)w2 + (size_t)(64 * kb + 16 * i) * (D_ * 2) + ft * 64, l0 + u * 4096 + i * 1024); }
        asm volatile("s_waitcnt vmcnt(0)" ::: "memory");
#pragma unroll
        for (int u = 0; u < 3; ++u) { const int un = 3 * gw + u, kb = un / 20, ft = un - kb * 20;
#pragma unroll
            for (int ks = 0; ks < 4; ++ks) { const lds_ptr a = my + u * 4096 + (16 * ks + 4 * h) * 64 + ro;
                ST16(ws + WS_W2 + (size_t)(ft * 108 + 4 * kb + ks) * 1024 + lane * 16, __builtin_bit_cast(u32x4, cat8(lds_tr(a), lds_tr(a + 512)))); } }
    } else {
#pragma unroll
        for (int u = 0; u < 2; ++u) { const int un = 2 * (gw - 180) + u, gp = un / 20, ft = un - gp * 20;
#pragma unroll
            for (int i = 0; i < 5; ++i) glds16(voff, (const char*)wo + (size_t)(80 * gp + 16 * i) * (D_ * 2) + ft * 64, l0 + u * 5120 + i * 1024); }
        asm volatile("s_waitcnt vmcnt(0)" ::: "memory");
#pragma unroll
        for (int u = 0; u < 2; ++u) { const int un = 2 * (gw - 180) + u, gp = un / 20, ft = un - gp * 20;
#pragma unroll
            for (int ks = 0; ks < 5; ++ks) {
                const lds_ptr a = my + u * 5120 + (ks < 4 ? 20 * ks + 4 * h : 40 * h + 16) * 64 + ro;
                ST16(ws + WS_WO + (size_t)(ft * 40 + 5 * gp + ks) * 1024 + lane * 16, __builtin_bit_cast(u32x4, cat8(lds_tr(a), lds_tr(a + (ks < 4 ? 512 : 1280))))); } }
    }
}

__global__ __launch_bounds__(768, 3) void k1_proj(const unsigned short* x, const unsigned short* wqkv, const unsigned short* w1, const unsigned short* b1,
                                                  const unsigned short* w2, const unsigned short* wo, char* ws) {
    extern __shared__ __attribute__((aligned(16))) char smem[];
    const lds_ptr lds = (lds_ptr)smem;
    const int lane = threadIdx.x & 63, wid = RFL(threadIdx.x >> 6);
    const int b = blockIdx.x;
    TS(0, 0);
#define RT_SLEEP 64
    if (b >= K1_GEMM_BLOCKS) {
        const int gw = (b - K1_GEMM_BLOCKS) * 12 + wid;
        const unsigned tag = epoch_tag(*(const unsigned*)(ws + WS_EP));
        u32x4 ra[8]; unsigned rb[8];
#pragma unroll
        for (int i = 0; i < 8; ++i) { const int row = gw + 288 * i < S_ ? gw + 288 * i : S_ - 1; const char* rp = (const char*)x + (size_t)row * (D_ * 2);
            ra[i] = *(const u32x4*)(rp + lane * 16); rb[i] = *(const unsigned*)(rp + 1024 + lane * 4); }
        u32x4 o = {0u, tag, 0u, tag};
#pragma unroll
        for (int i = 0; i < 8; ++i) { const f32x2 mv = row_stats(ra[i], rb[i]); if (lane == i) { o[0] = __float_as_uint(mv[0]); o[2] = __float_as_uint(mv[1]); } }
        if (lane < 8 && gw + 288 * lane < S_) ST16(ws + WS_ST + (size_t)(gw + 288 * lane) * 16, o);
        TS(0, 4);
        k1_retile(lds, gw, wid, lane, w2, wo, ws); TS(0, 5); return;
    }
    const int xcd = b & 7, t = 29 * (xcd & 3) + (b >> 3);
    const int rb = (xcd >> 2) * 4 + (t & 3), cb = t >> 2, row0 = rb * 256;
    const int g0 = cb < 28 ? cb : 29, g1 = cb < 27 ? cb : cb + 1;
    const char* wb = cb < 27 ? (const char*)w1 + g1 * 128 : (const char*)wqkv + g1 * 128;
    if (wid >= 8) {
        const int gw = b * 4 + wid - 8, l3 = lane < 9 ? lane : 0, kind0 = l3 / 3, i0 = gw + 928 * (l3 - 3 * kind0);
        const bool live = i0 < (kind0 == 0 ? 1920 : kind0 == 1 ? 1728 : 2048);
        const int kind = live ? kind0 : 0, idx = live ? i0 : gw;
        const int tstep = kind == 0 ? 64 * 3 * D_ * 2 : kind == 1 ? 64 * F_ * 2 : 128;
        const char* touch = (kind == 0 ? (const char*)wqkv : kind == 1 ? (const char*)w1 : (const char*)x) + idx * (kind == 2 ? D_ * 2 : 128) + 2 * tstep;
        const int xi = gw + 928 * (lane >> 3), xr = lane < 24 && xi < S_ ? xi : gw;
        const char* tx = (const char*)x + xr * (D_ * 2) + (2 + (lane & 7)) * 128;
        const int wi = gw + 928 * lane;
        const char* tw = wi < 17280 ? (const char*)w2 + wi * 128 : wi < 23680 ? (const char*)wo + (wi - 17280) * 128 : (const char*)w2 + gw * 128;
        k1_loader(lds, wid - 8, lane, row0, x, (const char*)wqkv + g0 * 128, 3 * D_ * 2, wb, cb < 27 ? F_ * 2 : 3 * D_ * 2, b1 + (cb < 27 ? g1 * 64 : 0), gw, ws, touch, tstep, tx, tw);
    } else {
        k1_compute(lds, wid, lane, row0, cb, g0, g1, x, ws);
    }
    TS(0, 7);
}

#define AT_SYNC WAITV_BAR(0)
#define AT_ROT 0
constexpr int AT_CH = 4 * 3072;
constexpr int AT_STAGE = 2 * AT_CH, AT_NST = 3, AT_LDS = AT_STAGE * AT_NST;

__device__ __forceinline__ f32x16 attn_fallback(const char* kvh, const char* q2, int lane) {
    const int r32 = lane & 31, h = lane >> 5;
    const bf16x8 qa = *(const bf16x8*)(q2 + lane * 16); bf16x4 qb = *(const bf16x4*)(q2 + 1024 + r32 * 8);
    if (h) qb = (bf16x4){0, 0, 0, 0};
    f32x16 o = f32x16{}; float m = -INFINITY;
    const int vrow = r32 < 21 ? r32 : 21;
#pragma unroll 1
    for (int t = 0; t < 64; ++t) {
        const char* kt = kvh + (size_t)t * KVT_B;
        const bf16x8 ka = *(const bf16x8*)(kt + lane * 16); const bf16x4 kb = *(const bf16x4*)(kt + 1024 + lane * 8);
        const bf16x8 v0 = *(const bf16x8*)(kt + 1536 + (h * 24 + vrow) * 16), v1 = *(const bf16x8*)(kt + 1536 + 768 + (h * 24 + vrow) * 16);
        f32x16 s = MFMA16(ka, qa, f32x16{}); s = MFMA8(kb, qb, s);
        float tm = s[0];
#pragma unroll
        for (int i = 1; i < 16; ++i) tm = __builtin_fmaxf(tm, s[i]);
        tm = xhalf_max(tm);
        const float mn = __builtin_fmaxf(m, tm);
        const float al = m == mn ? 1.0f : EX2(m - mn);
        m = mn;
#pragma unroll
        for (int i = 0; i < 16; ++i) { o[i] *= al; s[i] = EX2(s[i] - mn); }
        const u32x4 p0 = {pk2(s[0], s[1]), pk2(s[2], s[3]), pk2(s[4], s[5]), pk2(s[6], s[7])}, p1 = {pk2(s[8], s[9]), pk2(s[10], s[11]), pk2(s[12], s[13]), pk2(s[14], s[15])};
        o = MFMA16(v0, __builtin_bit_cast(bf16x8, p0), o); o = MFMA16(v1, __builtin_bit_cast(bf16x8, p1), o);
    }
    return o;
}

__device__ __forceinline__ void attn_block(lds_ptr lds, int bid, char* ws) {
    const int lane = threadIdx.x & 63, wid = RFL(threadIdx.x >> 6);
    const int r32 = lane & 31, h = lane >> 5;
    const int head = (bid & 7) * 4 + (bid >> 6), qblk = (bid >> 3) & 7;
    const int qg = wid & 3, kh = wid >> 2;
    const char* kvh = ws + WS_KV + (size_t)head * 64 * KVT_B;
    const unsigned l0 = (unsigned)(uintptr_t)lds;
    const char* src = kvh + (size_t)kh * 32 * KVT_B + qg * 3072;
    const unsigned dst = l0 + kh * AT_CH + qg * 3072, lo16 = lane * 16;
    auto issue = [&](int c, int slot) {
#pragma unroll
        for (int i = 0; i < 3; ++i) glds16(lo16, src + (size_t)((c + AT_ROT) & 7) * AT_CH + i * 1024, dst + slot * AT_STAGE + i * 1024);
    };
    const int qt0 = qblk * 8 + qg * 2;
    const char* qp = ws + WS_Q + (size_t)(head * 64 + qt0) * QT_B;
    bf16x8 qa0 = *(const bf16x8*)(qp + lane * 16), qa1 = *(const bf16x8*)(qp + QT_B + lane * 16);
    bf16x4 qb0 = *(const bf16x4*)(qp + 1024 + r32 * 8), qb1 = *(const bf16x4*)(qp + QT_B + 1024 + r32 * 8);
    { const bf16x8 ka = *(const bf16x8*)(kvh + lane * 16); const bf16x4 kb = *(const bf16x4*)(kvh + 1024 + lane * 8);
      issue(0, 0); issue(1, 1);
      if (h) { qb0 = (bf16x4){0, 0, 0, 0}; qb1 = (bf16x4){0, 0, 0, 0}; }
      f32x16 s0 = MFMA16(ka, qa0, f32x16{}); s0 = MFMA8(kb, qb0, s0);
      f32x16 s1 = MFMA16(ka, qa1, f32x16{}); s1 = MFMA8(kb, qb1, s1);
      float m0 = s0[0], m1 = s1[0];
#pragma unroll
      for (int i = 1; i < 16; ++i) { m0 = __builtin_fmaxf(m0, s0[i]); m1 = __builtin_fmaxf(m1, s1[i]); }
      m0 = xhalf_max(m0); m1 = xhalf_max(m1);
      if (h) { qb0[0] = (short)(pk2(-m0, 0.f) & 0xffffu); qb1[0] = (short)(pk2(-m1, 0.f) & 0xffffu); } }
    const int vrow = r32 < 21 ? r32 : 21;
    const lds_ptr kap = lds + kh * AT_CH + lane * 16, kbp = lds + kh * AT_CH + 1024 + lane * 8, vp = lds + kh * AT_CH + 1536 + (h * 24 + vrow) * 16;
    f32x16 o0 = f32x16{}, o1 = f32x16{}, s0, s1;
    u32x4 p0a, p0b, p1a = {0, 0, 0, 0}, p1b = {0, 0, 0, 0};
    bf16x8 ka, va = {0, 0, 0, 0, 0, 0, 0, 0}, vb = {0, 0, 0, 0, 0, 0, 0, 0}; bf16x4 kb;
    WAITV_BAR(3);
    TS(1, 1);
    ka = lds_r128(kap); kb = lds_r64(kbp);
    s0 = MFMA16(ka, qa0, f32x16{}); s0 = MFMA8(kb, qb0, s0);
#define G4(MF, X, i, P, j) do { MF; X[i] = EX2(X[i]); X[i + 1] = EX2(X[i + 1]); X[i + 2] = EX2(X[i + 2]); X[i + 3] = EX2(X[i + 3]); \
        P[j] = pk2(X[i], X[i + 1]); P[j + 1] = pk2(X[i + 2], X[i + 3]); PIN(P); SBAR(); } while (0)
    int slot = 0;
#pragma unroll 1
    for (int c = 0; c < 8; ++c) {
        const int sb = slot * AT_STAGE; const int nslot = slot == 2 ? 0 : slot + 1;
#pragma unroll
        for (int tt = 0; tt < 4; ++tt) {
#define LDK(o) do { ka = lds_r128(kap + (o)); kb = lds_r64(kbp + (o)); } while (0)
#define LDV(o) do { va = lds_r128(vp + (o)); vb = lds_r128(vp + (o) + 768); } while (0)
#define NEXTK() do { if (tt == 3) { if (c < 7) { AT_SYNC; if (c + 2 < 8) issue(c + 2, nslot == 2 ? 0 : nslot + 1); LDK(nslot * AT_STAGE); } } else LDK(sb + (tt + 1) * 3072); } while (0)
            G4(s1 = MFMA16(ka, qa1, f32x16{}), s0, 0, p0a, 0);
            G4(s1 = MFMA8(kb, qb1, s1), s0, 4, p0a, 2);
            NEXTK();
            G4(o1 = MFMA16(va, __builtin_bit_cast(bf16x8, p1a), o1), s0, 8, p0b, 0);
            G4(o1 = MFMA16(vb, __builtin_bit_cast(bf16x8, p1b), o1), s0, 12, p0b, 2);
            LDV(sb + tt * 3072);
            G4(s0 = MFMA16(ka, qa0, f32x16{}), s1, 0, p1a, 0);
            G4(s0 = MFMA8(kb, qb0, s0), s1, 4, p1a, 2);
            G4(o0 = MFMA16(va, __builtin_bit_cast(bf16x8, p0a), o0), s1, 8, p1b, 0);
            G4(o0 = MFMA16(vb, __builtin_bit_cast(bf16x8, p0b), o0), s1, 12, p1b, 2);
        }
        slot = nslot;
    }
    o1 = MFMA16(va, __builtin_bit_cast(bf16x8, p1a), o1); o1 = MFMA16(vb, __builtin_bit_cast(bf16x8, p1b), o1);
#undef G4
    TS(1, 2);
    BAR_LGKM();
    LAS f32x4* ex = (LAS f32x4*)lds + (wid * 3) * 64 + lane;
    { const f32x16 give = kh ? o0 : o1;
#pragma unroll
      for (int g = 0; g < 3; ++g) ex[g * 64] = (f32x4){give[4 * g], give[4 * g + 1], give[4 * g + 2], give[4 * g + 3]}; }
    BAR_LGKM();
    f32x16 o = kh ? o1 : o0;
    { const LAS f32x4* ey = (const LAS f32x4*)lds + ((wid ^ 4) * 3) * 64 + lane;
#pragma unroll
      for (int g = 0; g < 3; ++g) { const f32x4 a = ey[g * 64];
#pragma unroll
        for (int e = 0; e < 4; ++e) o[4 * g + e] += a[e]; } }
    float l = xhalf_sum(h ? o[8] : 0.f);
    float chk = 0.f;
#pragma unroll
    for (int i = 0; i < 12; ++i) chk = __builtin_fmaf(o[i], 0.f, chk);
    const bool bad = !(l > 8.6736174e-19f && l < 1.1529215e18f && chk == 0.f);
    if (__builtin_expect(__any(bad), 0)) {
        o = attn_fallback(kvh, qp + kh * QT_B, lane);
        l = xhalf_sum(h ? o[8] : 0.f);
    }
    const float rl = RCP(l);
    char* ob = ws + WS_O + (size_t)((qt0 + kh) * 40 + 5 * (head >> 2)) * 1024;
    *(u32x4*)(ob + (head & 3) * 1024 + lane * 16) = (u32x4){pk2(o[0] * rl, o[1] * rl), pk2(o[2] * rl, o[3] * rl), pk2(o[4] * rl, o[5] * rl), pk2(o[6] * rl, o[7] * rl)};
    if (!h) *(u32x2*)(ob + 4096 + ((head >> 1) & 1) * 512 + r32 * 16 + (head & 1) * 8) = (u32x2){pk2(o[8] * rl, o[9] * rl), pk2(o[10] * rl, o[11] * rl)};
}

#define M2_ROT 0
#define GLD(dst, voff, sbase, imm) asm volatile("global_load_dwordx4 %0, %1, %2 offset:" #imm : "=v"(dst) : "v"(voff), "s"(sbase) : "memory")
template <int NT>
__device__ __forceinline__ void mlp2_wave(lds_ptr red, int kq, int lane, const char* xs, const char* wsrc, const unsigned short* b2f, unsigned short* gout, int rot) {
    const int r32 = lane & 31, h = lane >> 5;
    unsigned vo[NT];
#pragma unroll
    for (int i = 0; i < NT; ++i) vo[i] = lane * 16 + i * 108 * 1024;
    f32x16 acc[NT];
#pragma unroll
    for (int i = 0; i < NT; ++i) acc[i] = f32x16{};
    u32x4 xf[3], wf[3][NT];
#define LD_STEP(r, imm) do { GLD(xf[r], vo[0], xs, imm); _Pragma("unroll") for (int i = 0; i < NT; ++i) GLD(wf[r][i], vo[i], wsrc, imm); } while (0)
#define USE_STEP(r) do { PIN(xf[r]); _Pragma("unroll") for (int i = 0; i < NT; ++i) { PIN(wf[r][i]); \
        acc[i] = MFMA16(__builtin_bit_cast(bf16x8, wf[r][i]), __builtin_bit_cast(bf16x8, xf[r]), acc[i]); } SBAR(); } while (0)
#define WAITN(n) asm volatile("s_waitcnt vmcnt(%0)" :: "i"(n) : "memory")
    xs += rot * 3072; wsrc += rot * 3072;
    LD_STEP(0, 0); LD_STEP(1, 1024); LD_STEP(2, 2048);
#pragma unroll 1
    for (int it = 0; it < 8; ++it) {
        const int adv = ++rot == 9 ? -8 * 3072 : 3072;
        xs += adv; wsrc += adv;
        WAITN(2 * (NT + 1)); USE_STEP(0); LD_STEP(0, 0);
        WAITN(2 * (NT + 1)); USE_STEP(1); LD_STEP(1, 1024);
        WAITN(2 * (NT + 1)); USE_STEP(2); LD_STEP(2, 2048);
    }
    WAITN(2 * (NT + 1)); USE_STEP(0); WAITN(NT + 1); USE_STEP(1); WAITN(0); USE_STEP(2);
    LAS f32x4* rb = (LAS f32x4*)red;
#pragma unroll
    for (int d = 1; d < 4; ++d) { const int dq = (kq + d) & 3;
#pragma unroll
        for (int i = 0; i < NT; ++i) {
            const f32x4 v = dq == 0 ? (f32x4){acc[i][0], acc[i][1], acc[i][2], acc[i][3]} : dq == 1 ? (f32x4){acc[i][4], acc[i][5], acc[i][6], acc[i][7]}
                          : dq == 2 ? (f32x4){acc[i][8], acc[i][9], acc[i][10], acc[i][11]} : (f32x4){acc[i][12], acc[i][13], acc[i][14], acc[i][15]};
            rb[((dq * 3 + (d - 1)) * NT + i) * 64 + lane] = v; } }
    BAR_LGKM();
#pragma unroll
    for (int i = 0; i < NT; ++i) {
        f32x4 v = kq == 0 ? (f32x4){acc[i][0], acc[i][1], acc[i][2], acc[i][3]} : kq == 1 ? (f32x4){acc[i][4], acc[i][5], acc[i][6], acc[i][7]}
                : kq == 2 ? (f32x4){acc[i][8], acc[i][9], acc[i][10], acc[i][11]} : (f32x4){acc[i][12], acc[i][13], acc[i][14], acc[i][15]};
#pragma unroll
        for (int d = 0; d < 3; ++d) v += rb[((kq * 3 + d) * NT + i) * 64 + lane];
        const int f = 32 * i + 8 * kq + 4 * h;
        const u32x2 bb = *(const u32x2*)(b2f + f);
        const float u0 = v[0] + bf_lo(bb[0]), u1 = v[1] + bf_hi(bb[0]), u2 = v[2] + bf_lo(bb[1]), u3 = v[3] + bf_hi(bb[1]);
#define SILU(u) ((u) * RCP(1.0f + EX2((u) * -LOG2E)))
        *(u32x2*)(gout + r32 * D_ + f) = (u32x2){pk2(SILU(u0), SILU(u1)), pk2(SILU(u2), SILU(u3))};
    }
}
__device__ __forceinline__ void mlp2_block(lds_ptr lds, int bid, const unsigned short* b2, char* ws) {
    const int lane = threadIdx.x & 63, wid = RFL(threadIdx.x >> 6), kq = wid & 3;
    const int tt = (bid & 7) * 8 + ((bid >> 3) & 7), cg = bid >> 6;
    const char* xs = ws + WS_H + (size_t)(tt * 108 + 27 * kq) * 1024;
    const int ft = 5 * cg + (wid < 4 ? 0 : 3);
    const char* wsrc = ws + WS_W2 + (size_t)(ft * 108 + 27 * kq) * 1024;
    unsigned short* gout = (unsigned short*)(ws + WS_G) + (size_t)tt * 32 * D_ + ft * 32;
    if (wid < 4) mlp2_wave<3>(lds, kq, lane, xs, wsrc, b2 + ft * 32, gout, M2_ROT);
    else mlp2_wave<2>(lds + 36864, kq, lane, xs, wsrc, b2 + ft * 32, gout, M2_ROT);
}

__global__ __launch_bounds__(512, 4) void k2_attn_mlp2(const unsigned short* b2, char* ws, int mode) {
    extern __shared__ __attribute__((aligned(16))) char smem[];
    const int b = blockIdx.x + mode;
    TS(1 + (mode != 0), 0);
    if (b < 256) attn_block((lds_ptr)smem, b, ws); else mlp2_block((lds_ptr)smem, b - 256, b2, ws);
    TS(1 + (mode != 0), 7);
}

#define K3_ROT 0
constexpr int K3_ROW = 336, K3_PART = 64 * K3_ROW, K3_LDS = 5 * K3_PART;
__global__ __launch_bounds__(320, 1) void k3_out(const unsigned short* x, char* ws, unsigned short* out) {
    TS(3, 0);
    extern __shared__ __attribute__((aligned(16))) char smem[];
    const lds_ptr lds = (lds_ptr)smem;
    const int lane = threadIdx.x & 63, wid = RFL(threadIdx.x >> 6), bid = blockIdx.x;
    const int xcd = bid & 7, i5 = bid >> 3;
    const int tb = RFL((xcd >> 1) * 8 + (i5 & 7)), fg = RFL((xcd & 1) * 4 + (i5 >> 3));
    const int c16 = lane & 15, g = lane >> 4;
    const unsigned vo = (g >> 1) * 1024 + (g & 1) * 512 + c16 * 16;
    const char* xs = uni(ws + WS_O + (size_t)(tb * 2 * 40 + 8 * wid) * 1024);
    const char* wsrc = uni(ws + WS_WO + (size_t)(8 * wid) * 1024);
    u32x4 xf[4][4], wf[4][5];
#pragma unroll
    for (int j0 = 0; j0 < 4; ++j0) {
        const int j = j0, jr = (j0 + K3_ROT) & 3;
#pragma unroll
        for (int i = 0; i < 5; ++i) { const int f16 = fg * 5 + i;
            asm volatile("global_load_dwordx4 %0, %1, %2" : "=v"(wf[j][i]) : "v"(vo), "s"(uni(wsrc + (size_t)((f16 >> 1) * 40 + 2 * jr) * 1024 + (f16 & 1) * 256)) : "memory"); }
#pragma unroll
        for (int t = 0; t < 4; ++t)
            asm volatile("global_load_dwordx4 %0, %1, %2" : "=v"(xf[j][t]) : "v"(vo), "s"(uni(xs + (size_t)((t >> 1) * 40 + 2 * jr) * 1024 + (t & 1) * 256)) : "memory");
    }
    const int t = threadIdx.x, tok = (t * 13108) >> 16, c = t - tok * 5;
    const size_t eo = (size_t)(tb * 64 + tok) * D_ + fg * 80 + c * 16;
    u32x4 xv[2], gv[2];
    { const unsigned short* gp = (const unsigned short*)(ws + WS_G);
      asm volatile("global_load_dwordx4 %0, %1, off" : "=v"(xv[0]) : "v"(x + eo) : "memory");
      asm volatile("global_load_dwordx4 %0, %1, off offset:16" : "=v"(xv[1]) : "v"(x + eo) : "memory");
      asm volatile("global_load_dwordx4 %0, %1, off" : "=v"(gv[0]) : "v"(gp + eo) : "memory");
      asm volatile("global_load_dwordx4 %0, %1, off offset:16" : "=v"(gv[1]) : "v"(gp + eo) : "memory"); }
    f32x4 acc[4][5];
#pragma unroll
    for (int a = 0; a < 4; ++a)
#pragma unroll
        for (int i = 0; i < 5; ++i) acc[a][i] = (f32x4){0.f, 0.f, 0.f, 0.f};
#pragma unroll
    for (int j = 0; j < 4; ++j) {
        asm volatile("s_waitcnt vmcnt(%0)" :: "i"(4 + 9 * (3 - j)) : "memory");
#pragma unroll
        for (int i = 0; i < 5; ++i) PIN(wf[j][i]);
#pragma unroll
        for (int a = 0; a < 4; ++a) { PIN(xf[j][a]);
#pragma unroll
            for (int i = 0; i < 5; ++i) acc[a][i] = __builtin_amdgcn_mfma_f32_16x16x32_bf16(__builtin_bit_cast(bf16x8, wf[j][i]), __builtin_bit_cast(bf16x8, xf[j][a]), acc[a][i], 0, 0, 0); }
        SBAR();
    }
#pragma unroll
    for (int a = 0; a < 4; ++a)
#pragma unroll
        for (int i = 0; i < 5; ++i) *(LAS f32x4*)(lds + wid * K3_PART + (16 * a + c16) * K3_ROW + (16 * i + 4 * g) * 4) = acc[a][i];
    TS(3, 1);
    WAITV_BAR(0);
    TS(3, 2);
    PIN(xv[0]); PIN(xv[1]); PIN(gv[0]); PIN(gv[1]);
#pragma unroll
    for (int j = 0; j < 2; ++j) {
        u32x4 o;
#pragma unroll
        for (int e = 0; e < 2; ++e) {
            f32x4 s = *(const LAS f32x4*)(lds + tok * K3_ROW + c * 64 + j * 32 + e * 16);
#pragma unroll
            for (int p = 1; p < 5; ++p) s += *(const LAS f32x4*)(lds + p * K3_PART + tok * K3_ROW + c * 64 + j * 32 + e * 16);
            o[2 * e] = pk2(__builtin_fmaf(s[0], bf_lo(gv[j][2 * e]), bf_lo(xv[j][2 * e])), __builtin_fmaf(s[1], bf_hi(gv[j][2 * e]), bf_hi(xv[j][2 * e])));
            o[2 * e + 1] = pk2(__builtin_fmaf(s[2], bf_lo(gv[j][2 * e + 1]), bf_lo(xv[j][2 * e + 1])), __builtin_fmaf(s[3], bf_hi(gv[j][2 * e + 1]), bf_hi(xv[j][2 * e + 1])));
        }
        *(u32x4*)(out + eo + j * 8) = o;
    }
    if (bid == 0 && threadIdx.x == 0) { unsigned* e = (unsigned*)(ws + WS_EP); *e = *e + 1u; }
    TS(3, 7);
}

extern "C" void kernel_launch(void* const* d_in, const int* in_sizes, int n_in, void* d_out, int out_size, void* d_ws, size_t ws_size, hipStream_t stream) {
    const unsigned short* x = (const unsigned short*)d_in[0]; const unsigned short* wqkv = (const unsigned short*)d_in[1];
    const unsigned short* wo = (const unsigned short*)d_in[2]; const unsigned short* w1 = (const unsigned short*)d_in[4];
    const unsigned short* b1 = (const unsigned short*)d_in[5]; const unsigned short* w2 = (const unsigned short*)d_in[6];
    const unsigned short* b2 = (const unsigned short*)d_in[7];
    char* ws = (char*)d_ws;
    (void)hipFuncSetAttribute((const void*)k1_proj, hipFuncAttributeMaxDynamicSharedMemorySize, K1_LDS);
    (void)hipFuncSetAttribute((const void*)k2_attn_mlp2, hipFuncAttributeMaxDynamicSharedMemorySize, AT_LDS);
    (void)hipFuncSetAttribute((const void*)k3_out, hipFuncAttributeMaxDynamicSharedMemorySize, K3_LDS);
    k1_proj<<<256, 768, K1_LDS, stream>>>(x, wqkv, w1, b1, w2, wo, ws);
    k2_attn_mlp2<<<512, 512, AT_LDS, stream>>>(b2, ws, 0);
    k3_out<<<256, 320, K3_LDS, stream>>>(x, ws, (unsigned short*)d_out);
}
```

```cpp
#include <hip/hip_runtime.h>
#include <hip/hip_bf16.h>
#include <stdint.h>

typedef short bf16x8 __attribute__((ext_vector_type(8)));
typedef short bf16x4 __attribute__((ext_vector_type(4)));
typedef float f32x16 __attribute__((ext_vector_type(16)));
typedef float f32x4 __attribute__((ext_vector_type(4)));
typedef float f32x2 __attribute__((ext_vector_type(2)));
typedef unsigned u32x4 __attribute__((ext_vector_type(4)));
typedef unsigned u32x2 __attribute__((ext_vector_type(2)));
typedef __bf16 bf16x2_t __attribute__((ext_vector_type(2)));
typedef short v4i16_t __attribute__((ext_vector_type(4)));
#define LAS __attribute__((address_space(3)))
typedef LAS char* lds_ptr;

constexpr int S_ = 2048, D_ = 640, F_ = 1728, NH = 32;
constexpr float LN_EPS = 1e-5f;
constexpr float LOG2E = 1.4426950408889634f;
constexpr float QSCALE = 0.22360679774997896f * LOG2E;

constexpr size_t QT_B = 1536, KVT_B = 3072;
constexpr size_t WS_Q = 0;
constexpr size_t WS_KV = WS_Q + (size_t)NH * 64 * QT_B;
constexpr size_t WS_H = WS_KV + (size_t)NH * 64 * KVT_B;
constexpr size_t WS_O = WS_H + (size_t)64 * 108 * 1024;
constexpr size_t WS_W2 = WS_O + (size_t)64 * 32 * 1280;
constexpr size_t WS_WO = WS_W2 + (size_t)20 * 108 * 1024;
constexpr size_t WS_ST = WS_WO + (size_t)20 * 40 * 1024;
constexpr size_t WS_EP = WS_ST + (size_t)S_ * 16 + 4096;
#define FLB 64
constexpr size_t WS_FL = WS_EP + 4096;
constexpr size_t WS_END = WS_FL + 2048 * FLB;

#define TS(k, slot) do {} while (0)
#define MFMA16(a, b, c) __builtin_amdgcn_mfma_f32_32x32x16_bf16(a, b, c, 0, 0, 0)
#define MFMA8(a, b, c) __builtin_amdgcn_mfma_f32_32x32x8bf16_1k(a, b, c, 0, 0, 0)
#define SBAR() __builtin_amdgcn_sched_barrier(0)
#define PIN(x) asm volatile("" : "+v"(x))
#define RFL(x) __builtin_amdgcn_readfirstlane(x)

__device__ __forceinline__ const char* uni(const void* p) {
    const unsigned long long a = (unsigned long long)p; return (const char*)(((unsigned long long)(unsigned)RFL((unsigned)(a >> 32)) << 32) | (unsigned)RFL((unsigned)a)); }
__device__ __forceinline__ unsigned pk2(float a, float b) { f32x2 v = {a, b}; return __builtin_bit_cast(unsigned, __builtin_convertvector(v, bf16x2_t)); }
__device__ __forceinline__ unsigned pk2v(f32x2 v) { return __builtin_bit_cast(unsigned, __builtin_convertvector(v, bf16x2_t)); }
__device__ __forceinline__ float bf_lo(unsigned u) { return __uint_as_float(u << 16); }
__device__ __forceinline__ float bf_hi(unsigned u) { return __uint_as_float(u & 0xffff0000u); }
__device__ __forceinline__ float xhalf_sum(float v) {
    auto r = __builtin_amdgcn_permlane32_swap(__float_as_uint(v), __float_as_uint(v), false, false);
    return __uint_as_float(r[0]) + __uint_as_float(r[1]); }
__device__ __forceinline__ float xhalf_max(float v) {
    auto r = __builtin_amdgcn_permlane32_swap(__float_as_uint(v), __float_as_uint(v), false, false);
    return __builtin_fmaxf(__uint_as_float(r[0]), __uint_as_float(r[1])); }
__device__ __forceinline__ void glds16(unsigned voff, const void* sbase, unsigned lds_base) {
    unsigned sv; asm volatile("s_mov_b32 %0, m0\n\ts_mov_b32 m0, %3\n\ts_nop 0\n\tglobal_load_lds_dwordx4 %1, %2\n\ts_mov_b32 m0, %0"
                              : "=&s"(sv) : "v"(voff), "s"(sbase), "s"(lds_base) : "memory"); }
__device__ __forceinline__ bf16x8 lds_r128(lds_ptr p) { return *(const LAS bf16x8*)p; }
__device__ __forceinline__ bf16x4 lds_r64(lds_ptr p) { return *(const LAS bf16x4*)p; }
__device__ __forceinline__ bf16x4 lds_tr(lds_ptr p) { return __builtin_bit_cast(bf16x4, __builtin_amdgcn_ds_read_tr16_b64_v4i16((LAS v4i16_t*)p)); }
__device__ __forceinline__ bf16x8 cat8(bf16x4 a, bf16x4 b) { return (bf16x8){a[0], a[1], a[2], a[3], b[0], b[1], b[2], b[3]}; }
#define WAITV_BAR(N) asm volatile("s_waitcnt vmcnt(" #N ") lgkmcnt(0)\n\ts_barrier" ::: "memory")
#define BAR_LGKM() asm volatile("s_waitcnt lgkmcnt(0)\n\ts_barrier" ::: "memory")
#define ST8(p, v) asm volatile("global_store_dwordx2 %0, %1, off sc1\n\ts_nop 1" :: "v"(p), "v"(v) : "memory")
#define EX2(v) __builtin_amdgcn_exp2f(v)
#define RCP(v) __builtin_amdgcn_rcpf(v)

constexpr int K1_STAGE = 49152, K1_NST = 3, K1_STATS = K1_STAGE * K1_NST, K1_LDS = K1_STATS + 4 * 1024 + 4 * 512 + 256 + 512 + 256 + 256;
constexpr int K1_GEMM_BLOCKS = 232;
#define TD 4
#define TMODE 0
#define EP_HOLD 14000
#define T_STG 0
#define T_X 0
#define T_W 0
#define T_K2 0
#define PF1_COND (b < 16 && wid < 6)
#define PF2 0
#define PF2_COND (b >= 256 && b < 272 && threadIdx.x < 192)
#define CODE_TOUCH(L, dump) do { unsigned long long pc_; int len_; \
        asm volatile("s_getpc_b64 %0\n" L ":\n\ts_mov_b32 %1, .Lcode_end-" L : "=s"(pc_), "=s"(len_)); \
        if ((int)threadIdx.x * 128 < len_) { unsigned sv_; asm volatile("s_mov_b32 %0, m0\n\ts_mov_b32 m0, %3\n\ts_nop 0\n\tglobal_load_lds_dword %1, %2\n\ts_mov_b32 m0, %0" \
            : "=&s"(sv_) : "v"(threadIdx.x * 128), "s"(pc_), "s"(dump) : "memory"); } } while (0)
#define PD 2
#define PS2 4
#define PRE_N 16
#define PRE_M 0x39
#define PSLEEP 16
#define ST_TRIES 64
__device__ __forceinline__ unsigned epoch_tag(unsigned e) { return e * 2654435761u + 0x7F4A7C15u; }
#define K1_ROT 0

#define TOUCH 0
#define STATS_MODE 0
#define ST16(p, v) asm volatile("global_store_dwordx4 %0, %1, off sc1\n\ts_nop 1" :: "v"(p), "v"(v) : "memory")
#define STP(p, v) (*(u32x4*)(p) = (v))
#define STN(p, v) __builtin_nontemporal_store((v), (u32x4*)(p))
#define STQ ST16
#define STH ST16
#define STW ST16
__device__ __forceinline__ void acc_sum(float& s, float v) { asm("v_add_f32 %0, %0, %1" : "+v"(s) : "v"(v)); }
__device__ __forceinline__ void acc_sq(float& s, float v) { asm("v_fmac_f32 %0, %1, %1" : "+v"(s) : "v"(v)); }
__device__ __forceinline__ float dot2(unsigned a, unsigned b, float c) {
    return __builtin_amdgcn_fdot2_f32_bf16(__builtin_bit_cast(bf16x2_t, a), __builtin_bit_cast(bf16x2_t, b), c, false); }

template <bool V0, bool V1>
__device__ __forceinline__ void k1_main(lds_ptr lds, int wf, int wt, int lane, f32x16 (&acc)[2][2]) {
    const int r32 = lane & 31, h = lane >> 5;
    unsigned xo[4][2], wa[4];
    { const int blk = (lane >> 4) & 1, q = (lane & 15) >> 2, p = lane & 3, ch = 4 * wf + 2 * blk + (p >> 1);
      const unsigned l0 = (unsigned)(uintptr_t)lds, a = l0 + 32768 + (8 * h + q) * 128 + 16 * (ch ^ ((q >> 1) << 2)) + 8 * (p & 1);
#pragma unroll
      for (int ks = 0; ks < 4; ++ks) { wa[ks] = a + (ks ^ wt) * 2048;
#pragma unroll
          for (int jp = 0; jp < 2; ++jp) { const int row = 64 * wt + 32 * (jp ^ wf) + r32; xo[ks][jp] = l0 + row * 128 + (((2 * (ks ^ wt) + h) ^ ((row >> 1) & 7)) << 4); } } }
#pragma unroll
    for (int a = 0; a < 2; ++a)
#pragma unroll
        for (int b = 0; b < 2; ++b) acc[a][b] = f32x16{};
    f32x4 c0 = {0.f, 0.f, 0.f, 0.f}, c1 = c0;
    const bf16x4 ones = {0x3F80, 0x3F80, 0x3F80, 0x3F80};
    bf16x8 fx0[2], fx1[2], fw0[2], fw1[2];
#define LP(a) ((lds_ptr)(uintptr_t)(a))
#define K1_LD(B, ks) do { fx0[B] = lds_r128(LP(xo[ks][0])); fw0[B] = cat8(lds_tr(LP(wa[ks])), lds_tr(LP(wa[ks]) + 512)); \
        fx1[B] = lds_r128(LP(xo[ks][1])); fw1[B] = cat8(lds_tr(LP(wa[ks]) + 8192), lds_tr(LP(wa[ks]) + 8192 + 512)); SBAR(); } while (0)
#define SUM4(c, f, o) do { c = __builtin_amdgcn_mfma_f32_4x4x4bf16_1k(ones, __builtin_shufflevector(f, f, o, o + 1, o + 2, o + 3), c, 0, 0, 0); SBAR(); } while (0)
#define K1_MM(B, ks) do { \
        acc[0][0] = V0 ? MFMA16(fx0[B], fw0[B], acc[0][0]) : MFMA16(fw0[B], fx0[B], acc[0][0]); SBAR(); if (ks == 0) SUM4(c0, fw0[B], 0); \
        acc[0][1] = V0 ? MFMA16(fx1[B], fw0[B], acc[0][1]) : MFMA16(fw0[B], fx1[B], acc[0][1]); SBAR(); if (ks == 0) SUM4(c0, fw0[B], 4); if (ks == 1) SUM4(c1, fw1[B], 0); \
        acc[1][0] = V1 ? MFMA16(fx0[B], fw1[B], acc[1][0]) : MFMA16(fw1[B], fx0[B], acc[1][0]); SBAR(); if (ks == 1) SUM4(c1, fw1[B], 4); \
        acc[1][1] = V1 ? MFMA16(fx1[B], fw1[B], acc[1][1]) : MFMA16(fw1[B], fx1[B], acc[1][1]); SBAR(); } while (0)
    BAR_LGKM();
    K1_LD(0, 0);
    int slot = 0;
#pragma unroll 1
    for (int s = 0; s < 9; ++s) {
        const int adv = slot == K1_NST - 1 ? -(K1_NST - 1) * K1_STAGE : K1_STAGE; slot = slot == K1_NST - 1 ? 0 : slot + 1;
        K1_LD(1, 1); xo[0][0] += adv; xo[0][1] += adv; wa[0] += adv; xo[1][0] += adv; xo[1][1] += adv; wa[1] += adv;
        K1_MM(0, 0);
        K1_LD(0, 2); xo[2][0] += adv; xo[2][1] += adv; wa[2] += adv;
        K1_MM(1, 1);
        K1_LD(1, 3); xo[3][0] += adv; xo[3][1] += adv; wa[3] += adv;
        K1_MM(0, 2);
        BAR_LGKM(); K1_LD(0, 0);
        K1_MM(1, 3);
    }
    K1_LD(1, 1); K1_MM(0, 0); K1_LD(0, 2); K1_MM(1, 1); K1_LD(1, 3);
    { const float a = xhalf_sum(c0[0]), b = xhalf_sum(c1[0]);
      ((LAS float*)(lds + K1_STATS))[1024 + wt * 128 + 64 * h + 32 * wf + r32] = h ? b : a; BAR_LGKM(); }
    K1_MM(0, 2); K1_MM(1, 3);
#undef K1_LD
#undef K1_MM
}

__device__ __forceinline__ void k1_store_v(const f32x16 (&acc)[2], const LAS float* s_r, const LAS float* s_cs, int wf, int wt, int lane, int row0, int grp, char* ws) {
    const int r32 = lane & 31, h = lane >> 5;
    const int fl = 32 * wf + r32, f = 64 * (grp - 20) + fl, hd = (f * 3277) >> 16, d = f - hd * 20;
    const float c = -s_cs[fl];
#pragma unroll
    for (int jp = 0; jp < 2; ++jp) {
        const int tl = 64 * wt + 32 * (jp ^ wf);
        char* base = ws + WS_KV + (size_t)(hd * 64 + ((row0 + tl) >> 5)) * KVT_B + 1536 + (h * 24 + d) * 16;
#pragma unroll
        for (int s = 0; s < 2; ++s) {
            u32x4 o;
#pragma unroll
            for (int gg = 0; gg < 2; ++gg) { const int g = 2 * s + gg;
                const f32x4 r4 = *(const LAS f32x4*)(s_r + tl + 8 * g + 4 * h), m4 = *(const LAS f32x4*)(s_r + 256 + tl + 8 * g + 4 * h);
#pragma unroll
                for (int e = 0; e < 2; ++e) { const f32x2 a = {acc[jp][4 * g + 2 * e], acc[jp][4 * g + 2 * e + 1]}, r2 = {r4[2 * e], r4[2 * e + 1]}, m2 = {m4[2 * e], m4[2 * e + 1]};
                    o[2 * gg + e] = pk2v(__builtin_elementwise_fma(a, r2, m2 * c)); } }
            STQ(base + s * 768, o);
            if (d >= 17) { const unsigned k = d == 19 ? 0x3F803F80u : 0u; const u32x4 kk = {d == 17 ? 0x3F80u : k, k, k, k};
                STQ(base + s * 768 + (d == 19 ? 16 : d == 18 ? 48 : 80), kk); }
        }
    }
}

__device__ __forceinline__ void k1_compute(lds_ptr lds, int wid, int lane, int row0, int cb, int g0, int g1, const unsigned short* x, char* ws) {
    const int wf = wid >> 2, wt = wid & 3, r32 = lane & 31, h = lane >> 5;
    f32x16 acc[2][2];
    if (cb < 20) k1_main<false, false>(lds, wf, wt, lane, acc);
    else if (cb < 27) k1_main<true, false>(lds, wf, wt, lane, acc);
    else k1_main<true, true>(lds, wf, wt, lane, acc);
    TS(0, 2);
    const LAS float* s_r = (const LAS float*)(lds + K1_STATS);
    const LAS float* s_b1 = s_r + 1536; const LAS float* s_cs = s_r + 1600;
    BAR_LGKM();
    TS(0, 3);
    if (cb < 20) {
        float rr[2], rm[2];
        const bool isq = cb < 10;
#pragma unroll
        for (int jp = 0; jp < 2; ++jp) { const int t = 64 * wt + 32 * (jp ^ wf) + r32; const float sc = isq ? QSCALE : 1.0f; rr[jp] = s_r[t] * sc; rm[jp] = -s_r[256 + t] * sc; }
        char* base = isq ? ws + WS_Q : ws + WS_KV; const int tb = isq ? (int)QT_B : (int)KVT_B;
#pragma unroll
        for (int gp = 0; gp < 2; ++gp) {
            const f32x4 ca = *(const LAS f32x4*)(s_cs + 32 * wf + 16 * gp + 4 * h), cc = *(const LAS f32x4*)(s_cs + 32 * wf + 16 * gp + 8 + 4 * h);
            const int G = 8 * (isq ? cb : cb - 10) + 4 * wf + 2 * gp + h, pr = (G * 205) >> 10, r = G - 5 * pr;
            const int off = (2 * pr + (r > 2)) * 64 * tb + (r == 2 ? 1024 : r == 1 || r == 4 ? 512 : 0) + r32 * 16;
#pragma unroll
            for (int jp = 0; jp < 2; ++jp) {
                const int tile = (row0 >> 5) + 2 * wt + (jp ^ wf);
                u32x4 o;
#pragma unroll
                for (int e = 0; e < 2; ++e) {
                    const f32x2 a0 = {acc[0][jp][8 * gp + 2 * e], acc[0][jp][8 * gp + 2 * e + 1]}, c0 = {ca[2 * e], ca[2 * e + 1]};
                    const f32x2 a1 = {acc[0][jp][8 * gp + 4 + 2 * e], acc[0][jp][8 * gp + 4 + 2 * e + 1]}, c1 = {cc[2 * e], cc[2 * e + 1]};
                    const auto sw = __builtin_amdgcn_permlane32_swap(pk2v(__builtin_elementwise_fma(a0, (f32x2){rr[jp], rr[jp]}, c0 * rm[jp])),
                                                                     pk2v(__builtin_elementwise_fma(a1, (f32x2){rr[jp], rr[jp]}, c1 * rm[jp])), false, false);
                    o[e] = sw[0]; o[2 + e] = sw[1]; }
                char* p = base + off + tile * tb;
                STQ(p, o);
                if (r == 2) STQ(p + 64 * tb, o);
            }
        }
    } else k1_store_v(acc[0], s_r, s_cs, wf, wt, lane, row0, g0, ws);
    TS(0, 1);
    if (cb < 27) {
        constexpr float C1 = -2.0f * LOG2E * 0.7978845608028654f, C2 = C1 * 0.044715f;
        float a2[2], m2[2];
#pragma unroll
        for (int jp = 0; jp < 2; ++jp) { const int t = 64 * wt + 32 * (jp ^ wf) + r32; a2[jp] = s_r[512 + t]; m2[jp] = -s_r[768 + t]; }
#pragma unroll
        for (int s = 0; s < 2; ++s) {
            u32x4 o[2];
#pragma unroll
            for (int gg = 0; gg < 2; ++gg) {
                const int g = 2 * s + gg, fl = 32 * wf + 8 * g + 4 * h;
                const f32x4 c4 = *(const LAS f32x4*)(s_cs + 64 + fl), b4 = *(const LAS f32x4*)(s_b1 + fl);
#pragma unroll
                for (int jp = 0; jp < 2; ++jp) {
#pragma unroll
                    for (int e = 0; e < 2; ++e) {
                        const f32x2 a = {acc[1][jp][4 * g + 2 * e], acc[1][jp][4 * g + 2 * e + 1]}, c2 = {c4[2 * e], c4[2 * e + 1]}, bb = {b4[2 * e], b4[2 * e + 1]};
                        const f32x2 u = __builtin_elementwise_fma(a, (f32x2){a2[jp], a2[jp]}, __builtin_elementwise_fma(c2, (f32x2){m2[jp], m2[jp]}, bb));
                        const f32x2 w = u * __builtin_elementwise_fma(u * u, (f32x2){C2, C2}, (f32x2){C1, C1});
                        const f32x2 d = (f32x2){EX2(w[0]), EX2(w[1])} + 1.0f;
                        o[jp][2 * gg + e] = pk2v(u * (f32x2){RCP(d[0]), RCP(d[1])});
                    }
                }
            }
#pragma unroll
            for (int jp = 0; jp < 2; ++jp) {
                const int tile = (row0 >> 5) + 2 * wt + (jp ^ wf);
                STH(ws + WS_H + (size_t)(tile * 108 + 4 * g1 + 2 * wf + s) * 1024 + lane * 16, o[jp]);
            }
        }
    } else if (cb == 27) k1_store_v(acc[1], s_r, s_cs + 64, wf, wt, lane, row0, g1, ws);
}

#define DPP_ADD(v, ctrl) v += __uint_as_float(__builtin_amdgcn_mov_dpp(__float_as_uint(v), ctrl, 0xf, 0xf, true))
__device__ __forceinline__ float wave_sum(float v) {
    DPP_ADD(v, 0xB1); DPP_ADD(v, 0x4E); DPP_ADD(v, 0x141); DPP_ADD(v, 0x140);
    auto r = __builtin_amdgcn_permlane16_swap(__float_as_uint(v), __float_as_uint(v), false, false);
    return xhalf_sum(__uint_as_float(r[0]) + __uint_as_float(r[1])); }

__device__ __forceinline__ f32x2 row_stats(u32x4 ra, unsigned rb) {
    float v[10];
#pragma unroll
    for (int j = 0; j < 4; ++j) { v[2 * j] = bf_lo(ra[j]); v[2 * j + 1] = bf_hi(ra[j]); }
    v[8] = bf_lo(rb); v[9] = bf_hi(rb);
    float a = 0.f;
#pragma unroll
    for (int j = 0; j < 10; ++j) a += v[j];
    const float m = wave_sum(a) * (1.0f / D_);
    a = 0.f;
#pragma unroll
    for (int j = 0; j < 10; ++j) a = __builtin_fmaf(v[j] - m, v[j] - m, a);
    return (f32x2){m, wave_sum(a) * (1.0f / D_)};
}

__device__ __forceinline__ void k1_loader(lds_ptr lds, int lw, int lane, int row0, const unsigned short* x, const char* w0, int ld0, const char* w1, int ld1,
                                          const unsigned short* b1g, int gw, char* ws, const char* touch, int tstep, const char* tx, const char* tw) {
    const unsigned l0 = (unsigned)(uintptr_t)lds;
    unsigned xv[2];
#pragma unroll
    for (int pp = 0; pp < 2; ++pp) { const int row = 8 * pp + (lane >> 3); xv[pp] = row * (D_ * 2) + (((lane & 7) ^ ((row >> 1) & 7)) << 4); }
    const int wc = ((lane & 7) ^ (((lane >> 4) & 1) << 2)) << 4;
    const char* wsrc = lw < 2 ? w0 : w1; const int ldw = lw < 2 ? ld0 : ld1;
    const unsigned wv = (lane >> 3) * ldw + wc;
    wsrc += (size_t)(lw & 1) * 32 * ldw;
    const char* xsrc = (const char*)x + (size_t)(row0 + 64 * lw) * (D_ * 2);
    auto issue = [&](int s, int slot) {
        const unsigned base = l0 + slot * K1_STAGE;
#pragma unroll
        for (int i = 0; i < 4; ++i) {
            glds16(wv, wsrc + (size_t)(s * 64 + 8 * i) * ldw, base + 32768 + (4 * lw + i) * 1024);
            glds16(xv[0], xsrc + i * 16 * (D_ * 2) + s * 128, base + (8 * lw + 2 * i) * 1024);
            glds16(xv[1], xsrc + i * 16 * (D_ * 2) + s * 128, base + (8 * lw + 2 * i + 1) * 1024);
        }
    };
    __builtin_amdgcn_s_setprio(3);
    const unsigned tdump = l0 + K1_LDS - 256;
#define K1_TCH(p) do { unsigned sv_; asm volatile("s_mov_b32 %0, m0\n\ts_mov_b32 m0, %2\n\ts_nop 0\n\tglobal_load_lds_dword %1, off\n\ts_mov_b32 m0, %0" \
        : "=&s"(sv_) : "v"(p), "s"(tdump) : "memory"); } while (0)
#define K1_TOUCH() do { if (T_STG) { K1_TCH(touch); touch += tstep; } } while (0)
#define NT_(c) (c)
    issue(0, 0);
    unsigned bv, ep; asm volatile("global_load_ushort %0, %1, %2" : "=v"(bv) : "v"(lane * 2), "s"(b1g) : "memory");
    const int tok = row0 + 64 * lw + lane;
    asm volatile("global_load_dword %0, %1, %2" : "=v"(ep) : "v"((tok % 288) * 4), "s"(ws + WS_EP) : "memory");
    issue(1, 1);
#pragma unroll
    for (int i = 2; i < TD; ++i) K1_TOUCH();
    u32x4 st;
    LAS float* s_r = (LAS float*)(lds + K1_STATS);
    const char* sp = ws + WS_ST + (size_t)tok * 16;
#pragma unroll
    for (int s = 0; s < 10; ++s) {
#define SX 0
#define SW 2
#define NTCH(s) ((s) < 0 ? T_STG * (TD - 2) : T_STG * ((s) < 10 - TD) + T_X * ((s) == SX) + T_W * ((s) == SW))
        const int newer = s == 9 ? 1 : 12 + NT_(NTCH(s - 1) + (s >= 1 ? NTCH(s - 2) : 0));
        asm volatile("s_waitcnt vmcnt(%0) lgkmcnt(0)\n\ts_barrier" :: "i"(newer) : "memory");
        if (s + 2 < 10) issue(s + 2, (s + 2) % K1_NST);
        if (s < 10 - TD) K1_TOUCH();
        if (T_X && s == SX) K1_TCH(tx);
        if (T_W && s == SW) K1_TCH(tw);
        if (s == 8) asm volatile("global_load_dwordx4 %0, %1, off sc1" : "=v"(st) : "v"(sp) : "memory");
    }
    asm volatile("s_waitcnt vmcnt(0)" : "+v"(st), "+v"(ep), "+v"(bv) :: "memory");
    unsigned tag = epoch_tag(ep);
    bool ok = st[1] == tag && st[3] == tag;
    auto finish = [&](float m, float var) {
        const float r = __builtin_amdgcn_rsqf(var + LN_EPS);
        const float a2 = r * __builtin_amdgcn_rsqf(__builtin_fmaf(var * r, r, LN_EPS));
        const int t = 64 * lw + lane;
        s_r[t] = r; s_r[256 + t] = r * m; s_r[512 + t] = a2; s_r[768 + t] = a2 * m; };
    finish(__uint_as_float(st[0]), __uint_as_float(st[2]));
    if (lw == 0) s_r[1536 + lane] = __uint_as_float(bv << 16);
    BAR_LGKM();
    if (lw < 2) { const LAS float* pc = s_r + 1024 + 64 * lw + lane; s_r[1600 + 64 * lw + lane] = (pc[0] + pc[128]) + (pc[256] + pc[384]); }
    if (__builtin_expect(!__all(ok), 0)) {
#pragma unroll 1
    for (int tr = 0; tr < ST_TRIES && !__all(ok); ++tr) {
        asm volatile("global_load_dwordx4 %0, %1, off sc1\n\ts_waitcnt vmcnt(0)" : "=v"(st) : "v"(sp) : "memory");
        ok = st[1] == tag && st[3] == tag;
    }
    float m = __uint_as_float(st[0]), var = __uint_as_float(st[2]);
    if (__builtin_expect(!__all(ok), 0)) {
#pragma unroll 1
        for (int i = 0; i < 64; ++i) { const char* rp = (const char*)x + (size_t)(row0 + 64 * lw + i) * (D_ * 2);
            const f32x2 mv = row_stats(*(const u32x4*)(rp + lane * 16), *(const unsigned*)(rp + 1024 + lane * 4));
            if (lane == i) { m = mv[0]; var = mv[1]; } }
    }
    finish(m, var);
    }
    BAR_LGKM();
}

__device__ __forceinline__ void k1_retile(lds_ptr lds, int gw, int wid, int lane, const unsigned short* w2, const unsigned short* wo, char* ws) {
    if (gw >= 260) return;
    const lds_ptr my = lds + wid * 12288; const unsigned l0 = (unsigned)(uintptr_t)my;
    const int h = lane >> 5, blk = (lane >> 4) & 1, q = (lane & 15) >> 2, p = lane & 3;
    const unsigned voff = (lane >> 2) * (D_ * 2) + (lane & 3) * 16;
    const unsigned ro = q * 64 + blk * 32 + p * 8;
    if (gw < 180) {
#pragma unroll
        for (int u = 0; u < 3; ++u) { const int un = 3 * gw + u, kb = un / 20, ft = un - kb * 20;
#pragma unroll
            for (int i = 0; i < 4; ++i) glds16(voff, (const char*)w2 + (size_t)(64 * kb + 16 * i) * (D_ * 2) + ft * 64, l0 + u * 4096 + i * 1024); }
        asm volatile("s_waitcnt vmcnt(0)" ::: "memory");
#pragma unroll
        for (int u = 0; u < 3; ++u) { const int un = 3 * gw + u, kb = un / 20, ft = un - kb * 20;
#pragma unroll
            for (int ks = 0; ks < 4; ++ks) { const lds_ptr a = my + u * 4096 + (16 * ks + 4 * h) * 64 + ro;
                STW(ws + WS_W2 + (size_t)(ft * 108 + 4 * kb + ks) * 1024 + lane * 16, __builtin_bit_cast(u32x4, cat8(lds_tr(a), lds_tr(a + 512)))); } }
    } else {
#pragma unroll
        for (int u = 0; u < 2; ++u) { const int un = 2 * (gw - 180) + u, gp = un / 20, ft = un - gp * 20;
#pragma unroll
            for (int i = 0; i < 5; ++i) glds16(voff, (const char*)wo + (size_t)(80 * gp + 16 * i) * (D_ * 2) + ft * 64, l0 + u * 5120 + i * 1024); }
        asm volatile("s_waitcnt vmcnt(0)" ::: "memory");
#pragma unroll
        for (int u = 0; u < 2; ++u) { const int un = 2 * (gw - 180) + u, gp = un / 20, ft = un - gp * 20;
#pragma unroll
            for (int ks = 0; ks < 5; ++ks) {
                const lds_ptr a = my + u * 5120 + (ks < 4 ? 20 * ks + 4 * h : 40 * h + 16) * 64 + ro;
                STW(ws + WS_WO + (size_t)(ft * 40 + 5 * gp + ks) * 1024 + lane * 16, __builtin_bit_cast(u32x4, cat8(lds_tr(a), lds_tr(a + (ks < 4 ? 512 : 1280))))); } }
    }
}

__global__ __launch_bounds__(768, 3) void k1_proj(const unsigned short* x, const unsigned short* wqkv, const unsigned short* w1, const unsigned short* b1,
                                                  const unsigned short* w2, const unsigned short* wo, char* ws) {
    extern __shared__ __attribute__((aligned(16))) char smem[];
    const lds_ptr lds = (lds_ptr)smem;
    const int lane = threadIdx.x & 63, wid = RFL(threadIdx.x >> 6);
    const int b = blockIdx.x;
    if (PF1_COND) CODE_TOUCH(".Lpf_k1", (unsigned)(uintptr_t)lds + K1_LDS - 256);
    TS(0, 0);
#define RT_SLEEP 64
    if (b >= K1_GEMM_BLOCKS) {
        const int gw = (b - K1_GEMM_BLOCKS) * 12 + wid;
        if (gw < 2 * FLB) { const u32x4 z = {0u, 0u, 0u, 0u}; ST16(ws + WS_FL + gw * 1024 + lane * 16, z); }
        const unsigned long long t0 = __builtin_readcyclecounter();
        const unsigned ep = *(const unsigned*)(ws + WS_EP + gw * 4), tag = epoch_tag(ep);
        u32x4 ra[8]; unsigned rb[8];
#pragma unroll
        for (int i = 0; i < 8; ++i) { const int row = gw + 288 * i < S_ ? gw + 288 * i : S_ - 1; const char* rp = (const char*)x + (size_t)row * (D_ * 2);
            ra[i] = *(const u32x4*)(rp + lane * 16); rb[i] = *(const unsigned*)(rp + 1024 + lane * 4); }
        u32x4 o = {0u, tag, 0u, tag};
#pragma unroll
        for (int i = 0; i < 8; ++i) { const f32x2 mv = row_stats(ra[i], rb[i]); if (lane == i) { o[0] = __float_as_uint(mv[0]); o[2] = __float_as_uint(mv[1]); } }
        if (lane < 8 && gw + 288 * lane < S_) ST16(ws + WS_ST + (size_t)(gw + 288 * lane) * 16, o);
        TS(0, 4);
        k1_retile(lds, gw, wid, lane, w2, wo, ws);
        TS(0, 5);
        while (__builtin_readcyclecounter() - t0 < EP_HOLD) __builtin_amdgcn_s_sleep(16);
        if (lane == 0) asm volatile("global_store_dword %0, %1, off sc1" :: "v"(ws + WS_EP + gw * 4), "v"(ep + 1u) : "memory");
        return;
    }
    const int xcd = b & 7, t = 29 * (xcd & 3) + (b >> 3);
    const int rb = (xcd >> 2) * 4 + (t & 3), cb = t >> 2, row0 = rb * 256;
    const int g0 = cb < 28 ? cb : 29, g1 = cb < 27 ? cb : cb + 1;
    const char* wb = cb < 27 ? (const char*)w1 + g1 * 128 : (const char*)wqkv + g1 * 128;
    if (wid >= 8) {
        const int gw = b * 4 + wid - 8, l3 = lane < 9 ? lane : 0, kind0 = l3 / 3, i0 = gw + 928 * (l3 - 3 * kind0);
        const bool live = i0 < (kind0 == 0 ? 1920 : kind0 == 1 ? 1728 : 2048);
        const int kind = live ? kind0 : 0, idx = live ? i0 : gw;
        const int tstep = kind == 0 ? 64 * 3 * D_ * 2 : kind == 1 ? 64 * F_ * 2 : 128;
        const char* touch = (kind == 0 ? (const char*)wqkv : kind == 1 ? (const char*)w1 : (const char*)x) + idx * (kind == 2 ? D_ * 2 : 128) + 2 * tstep;
        const int xi = gw + 928 * (lane >> 3), xr = lane < 24 && xi < S_ ? xi : gw;
        const char* tx = (const char*)x + xr * (D_ * 2) + (2 + (lane & 7)) * 128;
        const int wi = gw + 928 * lane;
        const char* tw = wi < 17280 ? (const char*)w2 + wi * 128 : wi < 23680 ? (const char*)wo + (wi - 17280) * 128 : (const char*)w2 + gw * 128;
        k1_loader(lds, wid - 8, lane, row0, x, (const char*)wqkv + g0 * 128, 3 * D_ * 2, wb, cb < 27 ? F_ * 2 : 3 * D_ * 2, b1 + (cb < 27 ? g1 * 64 : 0), gw, ws, touch, tstep, tx, tw);
    } else {
        k1_compute(lds, wid, lane, row0, cb, g0, g1, x, ws);
    }
    TS(0, 7);
}

#define AT_SYNC WAITV_BAR(0)
#define AT_ROT 0
constexpr int AT_CH = 4 * 3072;
constexpr int AT_STAGE = 2 * AT_CH, AT_NST = 3, AT_LDS = AT_STAGE * AT_NST;

__device__ __forceinline__ f32x16 attn_fallback(const char* kvh, const char* q2, int lane, int bo) {
    const int r32 = lane & 31, h = lane >> 5;
    const bf16x8 qa = *(const bf16x8*)(q2 + lane * 16); bf16x4 qb = *(const bf16x4*)(q2 + bo);
    if (h) qb = (bf16x4){0, 0, 0, 0};
    f32x16 o = f32x16{}; float m = -INFINITY;
    const int vrow = r32 < 21 ? r32 : 21;
#pragma unroll 1
    for (int t = 0; t < 64; ++t) {
        const char* kt = kvh + (size_t)t * KVT_B;
        const bf16x8 ka = *(const bf16x8*)(kt + lane * 16); const bf16x4 kb = *(const bf16x4*)(kt + bo);
        const bf16x8 v0 = *(const bf16x8*)(kt + 1536 + (h * 24 + vrow) * 16), v1 = *(const bf16x8*)(kt + 1536 + 768 + (h * 24 + vrow) * 16);
        f32x16 s = MFMA16(ka, qa, f32x16{}); s = MFMA8(kb, qb, s);
        float tm = s[0];
#pragma unroll
        for (int i = 1; i < 16; ++i) tm = __builtin_fmaxf(tm, s[i]);
        tm = xhalf_max(tm);
        const float mn = __builtin_fmaxf(m, tm);
        const float al = m == mn ? 1.0f : EX2(m - mn);
        m = mn;
#pragma unroll
        for (int i = 0; i < 16; ++i) { o[i] *= al; s[i] = EX2(s[i] - mn); }
        const u32x4 p0 = {pk2(s[0], s[1]), pk2(s[2], s[3]), pk2(s[4], s[5]), pk2(s[6], s[7])}, p1 = {pk2(s[8], s[9]), pk2(s[10], s[11]), pk2(s[12], s[13]), pk2(s[14], s[15])};
        o = MFMA16(v0, __builtin_bit_cast(bf16x8, p0), o); o = MFMA16(v1, __builtin_bit_cast(bf16x8, p1), o);
    }
    return o;
}

__device__ __forceinline__ void attn_block(lds_ptr lds, int bid, char* ws) {
    const int lane = threadIdx.x & 63, wid = RFL(threadIdx.x >> 6);
    const int r32 = lane & 31, h = lane >> 5;
    const int head = (bid & 7) * 4 + (bid >> 6), qblk = (bid >> 3) & 7;
    const int qg = wid & 3, kh = wid >> 2;
    const char* kvh = ws + WS_KV + (size_t)head * 64 * KVT_B;
    const unsigned l0 = (unsigned)(uintptr_t)lds;
    const char* src = kvh + (size_t)kh * 32 * KVT_B + qg * 3072;
    const unsigned dst = l0 + kh * AT_CH + qg * 3072, lo16 = lane * 16;
    auto issue = [&](int c, int slot) {
#pragma unroll
        for (int i = 0; i < 3; ++i) glds16(lo16, src + (size_t)((c + AT_ROT) & 7) * AT_CH + i * 1024, dst + slot * AT_STAGE + i * 1024);
    };
    const int qt0 = qblk * 8 + qg * 2;
    const char* qp = ws + WS_Q + (size_t)(head * 64 + qt0) * QT_B;
    bf16x8 qa0 = *(const bf16x8*)(qp + lane * 16), qa1 = *(const bf16x8*)(qp + QT_B + lane * 16);
    const int bo = 1024 + r32 * 16 + (head & 1) * 8;
    bf16x4 qb0 = *(const bf16x4*)(qp + bo), qb1 = *(const bf16x4*)(qp + QT_B + bo);
    { const bf16x8 ka = *(const bf16x8*)(kvh + lane * 16); const bf16x4 kb = *(const bf16x4*)(kvh + bo);
      issue(0, 0); issue(1, 1);
      if (h) { qb0 = (bf16x4){0, 0, 0, 0}; qb1 = (bf16x4){0, 0, 0, 0}; }
      f32x16 s0 = MFMA16(ka, qa0, f32x16{}); s0 = MFMA8(kb, qb0, s0);
      f32x16 s1 = MFMA16(ka, qa1, f32x16{}); s1 = MFMA8(kb, qb1, s1);
      float m0 = s0[0], m1 = s1[0];
#pragma unroll
      for (int i = 1; i < 16; ++i) { m0 = __builtin_fmaxf(m0, s0[i]); m1 = __builtin_fmaxf(m1, s1[i]); }
      m0 = xhalf_max(m0); m1 = xhalf_max(m1);
      if (h) { qb0[0] = (short)(pk2(-m0, 0.f) & 0xffffu); qb1[0] = (short)(pk2(-m1, 0.f) & 0xffffu); } }
    const int vrow = r32 < 21 ? r32 : 21;
    const lds_ptr kap = lds + kh * AT_CH + lane * 16, kbp = lds + kh * AT_CH + (h ? 1536 + 22 * 16 : bo), vp = lds + kh * AT_CH + 1536 + (h * 24 + vrow) * 16;
    f32x16 o0 = f32x16{}, o1 = f32x16{}, s0, s1;
    u32x4 p0a, p0b, p1a = {0, 0, 0, 0}, p1b = {0, 0, 0, 0};
    bf16x8 ka, va = {0, 0, 0, 0, 0, 0, 0, 0}, vb = {0, 0, 0, 0, 0, 0, 0, 0}; bf16x4 kb;
    WAITV_BAR(3);
    TS(1, 1);
    ka = lds_r128(kap); kb = lds_r64(kbp);
    s0 = MFMA16(ka, qa0, f32x16{}); s0 = MFMA8(kb, qb0, s0);
#define G4(MF, X, i, P, j) do { MF; X[i] = EX2(X[i]); X[i + 1] = EX2(X[i + 1]); X[i + 2] = EX2(X[i + 2]); X[i + 3] = EX2(X[i + 3]); \
        P[j] = pk2(X[i], X[i + 1]); P[j + 1] = pk2(X[i + 2], X[i + 3]); PIN(P); SBAR(); } while (0)
    int slot = 0;
#pragma unroll 1
    for (int c = 0; c < 8; ++c) {
        const int sb = slot * AT_STAGE; const int nslot = slot == 2 ? 0 : slot + 1;
#pragma unroll
        for (int tt = 0; tt < 4; ++tt) {
#define LDK(o) do { ka = lds_r128(kap + (o)); kb = lds_r64(kbp + (o)); } while (0)
#define LDV(o) do { va = lds_r128(vp + (o)); vb = lds_r128(vp + (o) + 768); } while (0)
#define NEXTK() do { if (tt == 3) { if (c < 7) { AT_SYNC; if (c + 2 < 8) issue(c + 2, nslot == 2 ? 0 : nslot + 1); LDK(nslot * AT_STAGE); } } else LDK(sb + (tt + 1) * 3072); } while (0)
            G4(s1 = MFMA16(ka, qa1, f32x16{}), s0, 0, p0a, 0);
            G4(s1 = MFMA8(kb, qb1, s1), s0, 4, p0a, 2);
            NEXTK();
            G4(o1 = MFMA16(va, __builtin_bit_cast(bf16x8, p1a), o1), s0, 8, p0b, 0);
            G4(o1 = MFMA16(vb, __builtin_bit_cast(bf16x8, p1b), o1), s0, 12, p0b, 2);
            LDV(sb + tt * 3072);
            G4(s0 = MFMA16(ka, qa0, f32x16{}), s1, 0, p1a, 0);
            G4(s0 = MFMA8(kb, qb0, s0), s1, 4, p1a, 2);
            G4(o0 = MFMA16(va, __builtin_bit_cast(bf16x8, p0a), o0), s1, 8, p1b, 0);
            G4(o0 = MFMA16(vb, __builtin_bit_cast(bf16x8, p0b), o0), s1, 12, p1b, 2);
        }
        slot = nslot;
    }
    o1 = MFMA16(va, __builtin_bit_cast(bf16x8, p1a), o1); o1 = MFMA16(vb, __builtin_bit_cast(bf16x8, p1b), o1);
#undef G4
    TS(1, 2);
    LAS f32x4* ex = (LAS f32x4*)(lds + 2 * AT_STAGE) + (wid * 3) * 64 + lane;
    { const f32x16 give = kh ? o0 : o1;
#pragma unroll
      for (int g = 0; g < 3; ++g) ex[g * 64] = (f32x4){give[4 * g], give[4 * g + 1], give[4 * g + 2], give[4 * g + 3]}; }
    BAR_LGKM();
    f32x16 o = kh ? o1 : o0;
    { const LAS f32x4* ey = (const LAS f32x4*)(lds + 2 * AT_STAGE) + ((wid ^ 4) * 3) * 64 + lane;
#pragma unroll
      for (int g = 0; g < 3; ++g) { const f32x4 a = ey[g * 64];
#pragma unroll
        for (int e = 0; e < 4; ++e) o[4 * g + e] += a[e]; } }
    float l = xhalf_sum(h ? o[8] : 0.f);
    float chk = 0.f;
#pragma unroll
    for (int i = 0; i < 12; ++i) chk = __builtin_fmaf(o[i], 0.f, chk);
    const bool bad = !(l > 8.6736174e-19f && l < 1.1529215e18f && chk == 0.f);
    if (__builtin_expect(__any(bad), 0)) {
        o = attn_fallback(kvh, qp + kh * QT_B, lane, bo);
        l = xhalf_sum(h ? o[8] : 0.f);
    }
    const float rl = RCP(l);
    char* ob = ws + WS_O + (size_t)((qt0 + kh) * 32 + head) * 1280;
    const u32x4 oa = {pk2(o[0] * rl, o[1] * rl), pk2(o[2] * rl, o[3] * rl), pk2(o[4] * rl, o[5] * rl), pk2(o[6] * rl, o[7] * rl)};
    u32x4 oc = {pk2(o[8] * rl, o[9] * rl), pk2(o[10] * rl, o[11] * rl), 0u, 0u};
    oc[2] = __builtin_amdgcn_mov_dpp(oc[0], 0xB1, 0xf, 0xf, true); oc[3] = __builtin_amdgcn_mov_dpp(oc[1], 0xB1, 0xf, 0xf, true);
    ST16(ob + lane * 16, oa);
    if (!(lane & 33)) ST16(ob + 1024 + r32 * 8, oc);
    asm volatile("s_waitcnt vmcnt(0)" ::: "memory");
    if (lane < FLB / 4) asm volatile("global_store_dword %0, %1, off sc1" :: "v"(ws + WS_FL + ((qt0 + kh) * 32 + head) * FLB + lane * 4), "v"(1u) : "memory");
}

constexpr int K2_LDS = 81920;
#define MFMA_S(a, b, c) __builtin_amdgcn_mfma_f32_16x16x32_bf16(__builtin_bit_cast(bf16x8, a), b, c, 0, 0, 0)
#define GLD(dst, voff, sbase) asm volatile("global_load_dwordx4 %0, %1, %2" : "=v"(dst) : "v"(voff), "s"(sbase) : "memory")
#define WAITN(n) asm volatile("s_waitcnt vmcnt(%0) lgkmcnt(0)\n\ts_barrier" :: "i"(n) : "memory")
template <int NT>
__device__ __forceinline__ void mlp_wave(lds_ptr lds, int wid, int lane, int tb, int f16, const unsigned short* x, const unsigned short* b2, char* ws, unsigned short* out) {
    const int kq = wid & 3, c16 = lane & 15, g = lane >> 4;
    const unsigned l0 = (unsigned)(uintptr_t)lds, lo16 = lane * 16;
    const unsigned vo = (g >> 1) * 1024 + (g & 1) * 512 + c16 * 16;
    unsigned wo_[NT];
#pragma unroll
    for (int i = 0; i < NT; ++i) wo_[i] = vo + ((f16 + i) & 1) * 256 + ((f16 + i) >> 1) * 108 * 1024;
    const char* hs = ws + WS_H + (size_t)(tb * 2 * 108 + wid) * 1024;
    const char* wsrc = ws + WS_W2 + (size_t)(2 * kq) * 1024;
    f32x4 acc[4][NT];
#pragma unroll
    for (int a = 0; a < 4; ++a)
#pragma unroll
        for (int i = 0; i < NT; ++i) acc[a][i] = (f32x4){0.f, 0.f, 0.f, 0.f};
    u32x4 wf[3][NT];
    auto ld_h = [&](int s) { const int o = (s == 13 && wid >= 4 ? 8 * s - 4 : 8 * s) * 1024;
        glds16(lo16, hs + o, l0 + (s & 3) * 16384 + wid * 1024); glds16(lo16, hs + o + 108 * 1024, l0 + (s & 3) * 16384 + 8192 + wid * 1024); };
#define LD_W(s) do { const int o_ = ((s) == 13 && kq >= 2 ? 8 * (s) - 4 : 8 * (s)) * 1024; _Pragma("unroll") for (int i = 0; i < NT; ++i) GLD(wf[(s) % 3][i], wo_[i], wsrc + o_); } while (0)
    ld_h(0); LD_W(0); ld_h(1); LD_W(1); ld_h(2); LD_W(2);
    constexpr int P0 = NT == 3 ? 0 : 9;
#define PIECE(d, i) (((P0 + ((d) - 1) * NT + (i)) >> 3) * 40960 + ((P0 + ((d) - 1) * NT + (i)) & 7) * 1024)
    lds_ptr rq[4];
#pragma unroll
    for (int d = 0; d < 4; ++d) { rq[d] = lds + ((kq - d) & 3) * 10240 + lo16; PIN(rq[d]); }
    unsigned qo[NT];
#pragma unroll
    for (int i = 0; i < NT; ++i) { qo[i] = vo + ((f16 + i) & 1) * 256 + ((f16 + i) >> 1) * 40 * 1024; PIN(qo[i]); }
    unsigned fo2 = (f16 * 16 + 4 * g) * 2; PIN(fo2);
    lds_ptr hp[4];
#pragma unroll
    for (int a = 0; a < 4; ++a) { const int t = (kq + a) & 3; hp[a] = lds + 2 * kq * 1024 + vo + (t >> 1) * 8192 + (t & 1) * 256; }
#pragma unroll
    for (int s = 0; s < 14; ++s) {
        WAITN((2 + NT) * (s < 12 ? 2 : 13 - s));
        if (s + 3 < 14) ld_h(s + 3);
        if (s < 13 || kq < 2) {
            bf16x8 xf[4];
#pragma unroll
            for (int a = 0; a < 4; ++a) xf[a] = lds_r128(hp[a] + (s & 3) * 16384);
#pragma unroll
            for (int i = 0; i < NT; ++i) PIN(wf[s % 3][i]);
#pragma unroll
            for (int a = 0; a < 4; ++a)
#pragma unroll
                for (int i = 0; i < NT; ++i) acc[a][i] = MFMA_S(wf[s % 3][i], xf[a], acc[a][i]);
        }
        SBAR();
        if (s + 3 < 14) LD_W(s + 3);
    }
    TS(2, 1);
    u32x4 wq[5][NT]; u32x2 bb[NT];
    const char* wob = ws + WS_WO + (size_t)(10 * kq) * 1024;
#pragma unroll
    for (int j = 0; j < 4; ++j)
#pragma unroll
        for (int i = 0; i < NT; ++i) GLD(wq[j][i], qo[i], wob + 2 * j * 1024);
#pragma unroll
    for (int i = 0; i < NT; ++i) asm volatile("global_load_dwordx2 %0, %1, %2 offset:%3" : "=v"(bb[i]) : "v"(fo2), "s"(b2), "i"(32 * i) : "memory");
    BAR_LGKM();
#pragma unroll
    for (int d = 1; d < 4; ++d)
#pragma unroll
        for (int i = 0; i < NT; ++i) *(LAS f32x4*)(rq[0] + PIECE(d, i)) = acc[d][i];
    BAR_LGKM();
#pragma unroll
    for (int d = 1; d < 4; ++d)
#pragma unroll
        for (int i = 0; i < NT; ++i) { acc[d][i] = *(const LAS f32x4*)(rq[d] + PIECE(d, i)); }
#pragma unroll
    for (int d = 1; d < 4; ++d)
#pragma unroll
        for (int i = 0; i < NT; ++i) PIN(acc[d][i]);
    const unsigned eo = (tb * 64 + 16 * kq + c16) * (D_ * 2) + fo2;
    if (wid == 0) { const char* fp = ws + WS_FL + (tb * 64 + (lane & PRE_M)) * FLB;
      for (;;) { unsigned f; asm volatile("global_load_dword %0, %1, off sc1\n\ts_waitcnt vmcnt(0)" : "=v"(f) : "v"(fp) : "memory");
          if (__builtin_popcountll(__ballot(f != 0u)) >= PRE_N) break;
          __builtin_amdgcn_s_sleep(PSLEEP); } }
    BAR_LGKM();
    TS(2, 2);
    const int qt = 2 * tb + (wid >> 2);
    { const char* fp = ws + WS_FL + (size_t)(qt * 32 + 8 * kq) * FLB; const unsigned pd = ((wid >> 2) * 40 + 10 * kq + 9) * 1024, fv = lane * FLB;
      unsigned seen = lane >= 8;
#define POLL(k) do { unsigned sv; if (!seen) asm volatile("s_mov_b32 %0, m0\n\ts_mov_b32 m0, %3\n\ts_nop 0\n\tglobal_load_lds_dword %1, %2 sc1\n\ts_mov_b32 m0, %0" \
                              : "=&s"(sv) : "v"(fv), "s"(fp), "s"(l0 + pd + (k) * 256) : "memory"); } while (0)
#pragma unroll
      for (int k = 0; k < PD - 1; ++k) { POLL(k); __builtin_amdgcn_s_sleep(PS2); }
      for (bool done = false; !done;) {
#pragma unroll
          for (int k = 0; k < PD; ++k) {
              POLL((k + PD - 1) % PD);
              asm volatile("s_waitcnt vmcnt(%0)" :: "i"(PD - 1) : "memory");
              if (!seen) seen = *(volatile LAS unsigned*)(lds + pd + k * 256 + lane * 4);
              if (__all(seen != 0u)) { done = true; break; }
              __builtin_amdgcn_s_sleep(PS2);
          } } }
    TS(2, 3);
#define OSC " sc1"
    { const char* os = ws + WS_O + (size_t)(qt * 32 + 8 * kq) * 1280; const unsigned od = l0 + ((wid >> 2) * 40 + 10 * kq) * 1024;
      const unsigned ro = ((lane >> 1) & 1) * 1280 + (lane >> 2) * 8 + (lane & 1) * 4;
#pragma unroll
      for (int gI = 0; gI < 2; ++gI) {
#pragma unroll
          for (int j = 0; j < 4; ++j) { unsigned sv; asm volatile("s_mov_b32 %0, m0\n\ts_mov_b32 m0, %3\n\ts_nop 0\n\tglobal_load_lds_dwordx4 %1, %2" OSC "\n\ts_mov_b32 m0, %0"
                              : "=&s"(sv) : "v"(lo16), "s"(os + (4 * gI + j) * 1280), "s"(od + (5 * gI + j) * 1024) : "memory"); }
#pragma unroll
          for (int j = 0; j < 4; ++j) { unsigned sv; asm volatile("s_mov_b32 %0, m0\n\ts_mov_b32 m0, %3\n\ts_nop 0\n\tglobal_load_lds_dword %1, %2" OSC "\n\ts_mov_b32 m0, %0"
                              : "=&s"(sv) : "v"(ro), "s"(os + (4 * gI + (j & 2)) * 1280 + 1024 + (j & 1) * 128), "s"(od + (5 * gI + 4) * 1024 + j * 256) : "memory"); }
      } }
    u32x2 gq[NT], xv[NT];
#pragma unroll
    for (int i = 0; i < NT; ++i) { PIN(bb[i]);
        const f32x4 v = (acc[0][i] + acc[1][i]) + (acc[2][i] + acc[3][i]);
        const float u0 = v[0] + bf_lo(bb[i][0]), u1 = v[1] + bf_hi(bb[i][0]), u2 = v[2] + bf_lo(bb[i][1]), u3 = v[3] + bf_hi(bb[i][1]);
#define SILU(u) ((u) * RCP(1.0f + EX2((u) * -LOG2E)))
        gq[i] = (u32x2){pk2(SILU(u0), SILU(u1)), pk2(SILU(u2), SILU(u3))}; PIN(gq[i]);
    }
    const f32x4 zero4 = {0.f, 0.f, 0.f, 0.f};
#pragma unroll
    for (int a = 0; a < 4; ++a) { const int t = (kq + a) & 3; hp[a] = lds + 10 * kq * 1024 + vo + (t >> 1) * 40960 + (t & 1) * 256; }
    SBAR();
#pragma unroll
    for (int i = 0; i < NT; ++i) GLD(wq[4][i], qo[i], wob + 8 * 1024);
#pragma unroll
    for (int i = 0; i < NT; ++i) asm volatile("global_load_dwordx2 %0, %1, %2 offset:%3" : "=v"(xv[i]) : "v"(eo), "s"(x), "i"(32 * i) : "memory");
#pragma unroll
    for (int j = 0; j < 5; ++j) {
        if (j == 0) { WAITN(12 + 2 * NT); TS(2, 4); }
        if (j == 2) WAITN(5 + 2 * NT);
        if (j == 4) WAITN(NT);
#pragma unroll
        for (int i = 0; i < NT; ++i) PIN(wq[j][i]);
#pragma unroll
        for (int ap = 0; ap < 2; ++ap) {
            const bf16x8 x0 = lds_r128(hp[2 * ap] + 2 * j * 1024), x1 = lds_r128(hp[2 * ap + 1] + 2 * j * 1024);
#pragma unroll
            for (int i = 0; i < NT; ++i) acc[2 * ap][i] = MFMA_S(wq[j][i], x0, j ? acc[2 * ap][i] : zero4);
#pragma unroll
            for (int i = 0; i < NT; ++i) acc[2 * ap + 1][i] = MFMA_S(wq[j][i], x1, j ? acc[2 * ap + 1][i] : zero4);
            if (j == 0) SBAR();
        }
        SBAR();
    }
    TS(2, 5);
#pragma unroll
    for (int d = 1; d < 4; ++d)
#pragma unroll
        for (int i = 0; i < NT; ++i) *(LAS f32x4*)(rq[0] + PIECE(d, i)) = acc[d][i];
    WAITN(0);
#pragma unroll
    for (int i = 0; i < NT; ++i) { PIN(xv[i]);
        const f32x4 v = (acc[0][i] + *(const LAS f32x4*)(rq[1] + PIECE(1, i))) + (*(const LAS f32x4*)(rq[2] + PIECE(2, i)) + *(const LAS f32x4*)(rq[3] + PIECE(3, i)));
        const u32x2 r = {pk2(__builtin_fmaf(v[0], bf_lo(gq[i][0]), bf_lo(xv[i][0])), __builtin_fmaf(v[1], bf_hi(gq[i][0]), bf_hi(xv[i][0]))),
                         pk2(__builtin_fmaf(v[2], bf_lo(gq[i][1]), bf_lo(xv[i][1])), __builtin_fmaf(v[3], bf_hi(gq[i][1]), bf_hi(xv[i][1])))};
        *(u32x2*)((char*)out + eo + 32 * i) = r;
    }
}

__global__ __launch_bounds__(512, 4) void k2_attn_mlp(const unsigned short* x, const unsigned short* b2, char* ws, unsigned short* out) {
    extern __shared__ __attribute__((aligned(16))) char smem[];
    const int b = blockIdx.x;
    if (b < 256) {
        TS(1, 0);
        attn_block((lds_ptr)smem, b, ws);
        TS(1, 7);
    } else {
        TS(2, 0);
#define MDELAY 0
        __builtin_amdgcn_s_sleep(MDELAY);
        const int lane = threadIdx.x & 63, wid = RFL(threadIdx.x >> 6);
        const int xcd = b & 7, i5 = (b - 256) >> 3;
        const int tb = (xcd >> 1) * 8 + (i5 & 7), fg = (xcd & 1) * 4 + (i5 >> 3);
        if (wid < 4) mlp_wave<3>((lds_ptr)smem, wid, lane, tb, fg * 5, x, b2, ws, out);
        else mlp_wave<2>((lds_ptr)smem, wid, lane, tb, fg * 5 + 3, x, b2, ws, out);
        TS(2, 7);
    }
}

__global__ void k3_mark() { asm volatile(".Lcode_end:"); }

extern "C" void kernel_launch(void* const* d_in, const int* in_sizes, int n_in, void* d_out, int out_size, void* d_ws, size_t ws_size, hipStream_t stream) {
    const unsigned short* x = (const unsigned short*)d_in[0]; const unsigned short* wqkv = (const unsigned short*)d_in[1];
    const unsigned short* wo = (const unsigned short*)d_in[2]; const unsigned short* w1 = (const unsigned short*)d_in[4];
    const unsigned short* b1 = (const unsigned short*)d_in[5]; const unsigned short* w2 = (const unsigned short*)d_in[6];
    const unsigned short* b2 = (const unsigned short*)d_in[7];
    char* ws = (char*)d_ws;
    (void)hipFuncSetAttribute((const void*)k1_proj, hipFuncAttributeMaxDynamicSharedMemorySize, K1_LDS);
    (void)hipFuncSetAttribute((const void*)k2_attn_mlp, hipFuncAttributeMaxDynamicSharedMemorySize, K2_LDS);
    k1_proj<<<256, 768, K1_LDS, stream>>>(x, wqkv, w1, b1, w2, wo, ws);
    k2_attn_mlp<<<512, 512, K2_LDS, stream>>>(x, b2, ws, (unsigned short*)d_out);
}
```
